```python
import torch

def _softmax(x, axis=-1):
    return torch.softmax(x, dim=axis)
def _gelu(x):
    return torch.nn.functional.gelu(x, approximate="tanh")
def _silu(x):
    return torch.nn.functional.silu(x)
def _sigmoid(x):
    return torch.sigmoid(x)
def _layernorm(x, axis=-1, eps=1e-5):
    m = x.mean(dim=axis, keepdim=True)
    v = x.var(dim=axis, keepdim=True, unbiased=False)
    return (x - m) / torch.sqrt(v + eps)
def _scatter_add(tgt, idx, src, axis=0):
    return tgt.clone().index_add_(axis, idx, src)
def _segment_sum(src, seg, n):
    out = torch.zeros((n,) + src.shape[1:], dtype=src.dtype)
    return out.index_add_(0, seg, src)
def _pad(x, pw):
    flat = tuple(v for pair in reversed(pw) for v in pair)
    return torch.nn.functional.pad(x, flat)

kernel_name = 'gather_scatter__edge_featured_mpnn_egc__graph'

def setup_inputs():

    _w = (lambda a: (lambda t: t.to(torch.bfloat16) if t.is_floating_point() else t)(torch.as_tensor(a)))
    N, deg, D, De = 10000, 16, 256, max(16, 1)
    E = N * deg
    return {
        "x": _w((lambda *s: torch.randn(*s, dtype=torch.bfloat16) * 0.1)(N, D)),
        "e_attr": _w((lambda *s: torch.randn(*s, dtype=torch.bfloat16) * 0.1)(E, De)),
        "src": _w((lambda lo, hi, *s: torch.randint(lo, hi, s))(0, N, E).to(torch.int64)),
        "dst": _w((lambda lo, hi, *s: torch.randint(lo, hi, s))(0, N, E).to(torch.int64)),
        "u_w": _w((lambda *s: torch.randn(*s, dtype=torch.bfloat16) * 0.05)(D, D)),
        "v_w": _w((lambda *s: torch.randn(*s, dtype=torch.bfloat16) * 0.05)(D, D)),
        "g_w": _w((lambda *s: torch.randn(*s, dtype=torch.bfloat16) * 0.1)(2 * D + De, D)),
    }

def reference(x, e_attr, src, dst, u_w, v_w, g_w):
    N, D = x.shape
    g_in = torch.concatenate([x[dst], x[src], e_attr], axis=-1)
    eta = _sigmoid(g_in @ g_w)
    hv = x @ v_w
    msg = eta * hv[src]
    agg = _segment_sum(msg, dst, N)
    return _silu(x @ u_w + agg)
```

```cpp
#include <hip/hip_runtime.h>
#include <hip/hip_bf16.h>
#include <stdint.h>

namespace egc {

typedef unsigned int u32;
typedef unsigned short u16;
typedef float f32x2 __attribute__((ext_vector_type(2)));
typedef float f32x16 __attribute__((ext_vector_type(16)));
typedef __bf16 bf16x2 __attribute__((ext_vector_type(2)));
typedef __bf16 bf16x4 __attribute__((ext_vector_type(4)));
typedef __bf16 bf16x8 __attribute__((ext_vector_type(8)));
typedef short s16x4 __attribute__((ext_vector_type(4)));
typedef u32 u32x2 __attribute__((ext_vector_type(2)));
typedef u32 u32x4 __attribute__((ext_vector_type(4)));
#define LDSP __attribute__((address_space(3)))

constexpr int N = 10000, D = 256, DE = 16, E = 160000;
constexpr int NPROD = 64;
constexpr int CHUNK = E / NPROD;
constexpr int NTILE = 512;
constexpr int TPG = 8;
constexpr int OFFP = 520;
constexpr int NROWP = 10016;
constexpr size_t SLICEB = (size_t)NROWP * 256;
constexpr int CAPT = 448;
constexpr float NLOG2E = -1.44269504088896340736f;

constexpr size_t WS_QV = 0;
constexpr size_t WS_PU = WS_QV + 4 * SLICEB;
constexpr size_t WS_EP = WS_PU + 4 * SLICEB;
constexpr size_t WS_REC = WS_EP + (size_t)(E + 64) * 32;
constexpr size_t WS_OFF = WS_REC + (size_t)(E + 64) * 4;
constexpr size_t WS_END = WS_OFF + (size_t)NPROD * OFFP * 2;

__device__ __forceinline__ int tile_start(int t) { return (625 * t) >> 5; }
__device__ __forceinline__ int tile_of(int d) { return (32 * d + 31) / 625; }

__device__ __forceinline__ f32x16 mfma(bf16x8 a, bf16x8 b, f32x16 c) {
  return __builtin_amdgcn_mfma_f32_32x32x16_bf16(a, b, c, 0, 0, 0);
}
__device__ __forceinline__ u32 pack_bf16(float a, float b) {
  bf16x2 v;
  v[0] = (__bf16)a;
  v[1] = (__bf16)b;
  return __builtin_bit_cast(u32, v);
}
__device__ __forceinline__ float bf16_bits_to_f32(u16 b) { return __builtin_bit_cast(float, (u32)b << 16); }

__device__ __forceinline__ void dma16(const void* sbase, u32 voff, u32 lds) {
  asm volatile("s_mov_b32 m0, %2\n\ts_nop 0\n\tglobal_load_lds_dwordx4 %0, %1"
               :
               : "v"(voff), "s"(sbase), "s"(lds)
               : "memory");
}
__device__ __forceinline__ void mem_fence() { asm volatile("" ::: "memory"); }
__device__ __forceinline__ void wait_vm0() { asm volatile("s_waitcnt vmcnt(0)" ::: "memory"); }
__device__ __forceinline__ void wait_lgkm0() { asm volatile("s_waitcnt lgkmcnt(0)" ::: "memory"); }
__device__ __forceinline__ void wg_barrier() {
  asm volatile("s_waitcnt lgkmcnt(0)\n\ts_barrier" ::: "memory");
}
__device__ __forceinline__ bf16x8 lds_read16(u32 addr) { return *(const LDSP bf16x8*)(size_t)addr; }
__device__ __forceinline__ u32x4 lds_read16u(u32 addr) { return *(const LDSP u32x4*)(size_t)addr; }
__device__ __forceinline__ s16x4 lds_read_tr(u32 addr) {
  return __builtin_amdgcn_ds_read_tr16_b64_v4i16((LDSP s16x4*)(size_t)addr);
}
__device__ __forceinline__ bf16x8 join8(s16x4 lo, s16x4 hi) {
  u32x2 a = __builtin_bit_cast(u32x2, lo), b = __builtin_bit_cast(u32x2, hi);
  u32x4 r = {a.x, a.y, b.x, b.y};
  return __builtin_bit_cast(bf16x8, r);
}

constexpr int K1_LDS = 81920;

__device__ __forceinline__ void producer_block(char* smem, int pb, const int* __restrict__ src,
                                               const int* __restrict__ dst, const char* __restrict__ eattr,
                                               char* __restrict__ ws) {
  u32* hist = (u32*)smem;
  u32* wsum = hist + 512;
  u32* srec = wsum + 16;
  u16* seid = (u16*)(srec + 2560);
  const int tid = threadIdx.x, lane = tid & 63, w = tid >> 6;
  const int e0 = pb * CHUNK;
  hist[tid] = 0;
  int dd[5], ss[5];
#pragma unroll
  for (int k = 0; k < 5; ++k) {
    const int i = tid + 512 * k;
    const int ii = i < CHUNK ? i : 0;
    dd[k] = dst[e0 + ii];
    ss[k] = src[e0 + ii];
  }
  __syncthreads();
  u32 rec[5], tl[5], rk[5];
#pragma unroll
  for (int k = 0; k < 5; ++k) {
    const int i = tid + 512 * k;
    const int t = tile_of(dd[k]);
    const int dl = dd[k] - tile_start(t);
    rec[k] = ((u32)ss[k] << 8) | ((u32)dl << 22) | ((u32)(t & 7) << 27);
    tl[k] = t;
    rk[k] = 0;
    if (i < CHUNK) rk[k] = atomicAdd(&hist[t], 1u);
  }
  __syncthreads();
  const u32 c = hist[tid];
  u32 inc = c;
#pragma unroll
  for (int o = 1; o < 64; o <<= 1) {
    const u32 v = __shfl_up(inc, o, 64);
    if (lane >= o) inc += v;
  }
  if (lane == 63) wsum[w] = inc;
  __syncthreads();
  u32 wb = 0;
#pragma unroll
  for (int j = 0; j < 8; ++j) wb += j < w ? wsum[j] : 0u;
  const u32 base = wb + inc - c;
  hist[tid] = base;
  u16* off = (u16*)(ws + WS_OFF) + pb * OFFP;
  off[tid] = (u16)base;
  if (tid < OFFP - 512) off[512 + tid] = (u16)CHUNK;
  __syncthreads();
#pragma unroll
  for (int k = 0; k < 5; ++k) {
    const int i = tid + 512 * k;
    if (i < CHUNK) {
      const u32 p = hist[tl[k]] + rk[k];
      srec[p] = rec[k];
      seid[p] = (u16)i;
    }
  }
  __syncthreads();
  u32* grec = (u32*)(ws + WS_REC) + e0;
#pragma unroll
  for (int k = 0; k < 5; ++k) {
    const int i = tid + 512 * k;
    if (i < CHUNK) grec[i] = srec[i];
  }
  const char* ein = eattr + (size_t)e0 * 32;
  char* eout = ws + WS_EP + (size_t)e0 * 32;
#pragma unroll
  for (int k = 0; k < 10; ++k) {
    const int i = tid + 512 * k;
    if (i < 2 * CHUNK) {
      const u32x4 v = *(const u32x4*)(ein + (int)seid[i >> 1] * 32 + (i & 1) * 16);
      *(u32x4*)(eout + i * 16) = v;
    }
  }
  if (pb == 0) {
    const u32x4 z = {0, 0, 0, 0};
    if (tid < 64) *(u32x4*)(ws + WS_QV + (tid >> 4) * SLICEB + (size_t)N * 256 + (tid & 15) * 16) = z;
    if (tid >= 64 && tid < 66) *(u32x4*)(ws + WS_EP + (size_t)E * 32 + (tid - 64) * 16) = z;
  }
}

__device__ __forceinline__ void gemm_block(char* smem, int gb, const char* __restrict__ x,
                                           const char* __restrict__ uw, const char* __restrict__ vw,
                                           const char* __restrict__ gw, char* __restrict__ ws) {
  const int tid = threadIdx.x, lane = tid & 63;
  const int w = __builtin_amdgcn_readfirstlane(tid >> 6);
  const u32 lds0 = (u32)(size_t)(LDSP char*)smem;
  const int q = gb >> 3, xcd = gb & 7;
  const int type = q & 3;
  const int rt0 = (q >> 2) * 8 + xcd;
  const char* W = type == 0 ? gw + 256 * 512 : type == 1 ? vw : type == 2 ? gw : uw;
  const float scale = (type & 1) ? 1.0f : NLOG2E;
  char* dest = ws + (type < 2 ? WS_QV : WS_PU) + (type & 1) * 128;

  const u32 XB0 = 65536, XB1 = 0, TB = 16384;
  auto load_x = [&](int rt, u32 xb) {
#pragma unroll
    for (int i = 0; i < 2; ++i) {
      const int j = w * 2 + i;
      const int row = 2 * j + (lane >> 5);
      int node = rt * 32 + row;
      node = node < N ? node : N - 1;
      const u32 voff = (u32)node * 512u + (u32)(((lane & 31) ^ (row & 15)) * 16);
      dma16(x, voff, lds0 + xb + j * 1024);
    }
  };
  auto load_w = [&](int hf) {
#pragma unroll
    for (int i = 0; i < 8; ++i) {
      const int j = w * 8 + i;
      const int k = 2 * j + (lane >> 5);
      const u32 voff = (u32)(hf * 128 + k) * 512u + (u32)(((lane & 31) ^ ((k & 3) << 2)) * 16);
      dma16(W, voff, lds0 + j * 1024);
    }
  };
  bf16x8 wf[16];
  const int h = lane >> 5, blk = (lane >> 4) & 1, qq = (lane & 15) >> 2, p = lane & 3;
  const int col0 = w * 32 + 16 * (p & 1) + 8 * blk + 4 * (p >> 1);
  auto read_w = [&](int hf) {
#pragma unroll
    for (int ks = 0; ks < 8; ++ks) {
      s16x4 part[2];
#pragma unroll
      for (int t = 0; t < 2; ++t) {
        const int k = ks * 16 + 8 * h + 4 * t + qq;
        const u32 a = lds0 + k * 512 + ((((col0 >> 3) ^ ((k & 3) << 2)) << 4) | ((col0 & 7) << 1));
        part[t] = lds_read_tr(a);
      }
      wf[hf * 8 + ks] = join8(part[0], part[1]);
    }
  };

  load_w(0);
  load_x(rt0, XB0);
  wait_vm0();
  wg_barrier();
  read_w(0);
  wg_barrier();
  load_w(1);
  wait_vm0();
  wg_barrier();
  read_w(1);
  wg_barrier();

  const int n = lane & 31;
#pragma unroll 1
  for (int it = 0; it < 5; ++it) {
    const int rt = rt0 + 64 * it;
    if (rt >= 313) break;
    const u32 xb = (it & 1) ? XB1 : XB0;
    const int rtn = rt + 64;
    if (it < 4 && rtn < 313) load_x(rtn, (it & 1) ? XB0 : XB1);
    f32x16 acc = {0};
#pragma unroll
    for (int ks = 0; ks < 16; ++ks) {
      const bf16x8 xf = lds_read16(lds0 + xb + n * 512 + (((2 * ks + h) ^ (n & 15)) << 4));
      acc = mfma(wf[ks], xf, acc);
    }
    u32x4 c0, c1;
#pragma unroll
    for (int i = 0; i < 4; ++i) {
      c0[i] = pack_bf16(acc[2 * i] * scale, acc[2 * i + 1] * scale);
      c1[i] = pack_bf16(acc[8 + 2 * i] * scale, acc[8 + 2 * i + 1] * scale);
    }
    const u32 tb = lds0 + TB + w * 2048;
    const int sw = (n >> 1) & 3;
    *(LDSP u32x4*)(size_t)(tb + n * 64 + (((2 * h) ^ sw) << 4)) = c0;
    *(LDSP u32x4*)(size_t)(tb + n * 64 + (((2 * h + 1) ^ sw) << 4)) = c1;
    wait_lgkm0();
#pragma unroll
    for (int i = 0; i < 2; ++i) {
      const int nn = 16 * i + (lane >> 2), ch = lane & 3;
      const u32x4 v = lds_read16u(tb + nn * 64 + ((ch ^ ((nn >> 1) & 3)) << 4));
      const int node = rt * 32 + nn;
      if (node < N)
        *(u32x4*)(dest + (size_t)(w >> 1) * SLICEB + (size_t)node * 256 + (w & 1) * 64 + ch * 16) = v;
    }
    wait_vm0();
    wg_barrier();
  }
}

__global__ void __launch_bounds__(512, 4)
prep_kernel(const char* __restrict__ x, const char* __restrict__ eattr, const int* __restrict__ src,
            const int* __restrict__ dst, const char* __restrict__ uw, const char* __restrict__ vw,
            const char* __restrict__ gw, char* __restrict__ ws) {
  extern __shared__ __attribute__((aligned(16))) char smem[];
  const int b = blockIdx.x;
  if (b < NPROD)
    producer_block(smem, b, src, dst, eattr, ws);
  else
    gemm_block(smem, b - NPROD, x, uw, vw, gw, ws);
}

constexpr u32 L_QV = 0;
constexpr u32 L_E = 65536;
constexpr u32 L_S = L_E + 8 * 1024;
constexpr u32 L_A1 = L_S + 8 * 2048 + 128;
constexpr u32 L_A2 = L_A1 + TPG * CAPT * 4;
constexpr u32 L_A3 = L_A2 + TPG * CAPT * 4;
constexpr u32 L_DL = L_A3 + TPG * CAPT * 2;
constexpr u32 L_GS = L_DL + 8 * 64 * 4;
constexpr u32 L_GE = L_GS + 256;
constexpr u32 L_TT = L_GE + 256;
constexpr u32 K2_LDS = L_TT + 64;

__global__ void __launch_bounds__(512, 2)
edge_kernel(const u16* __restrict__ gw, const char* __restrict__ ws, u16* __restrict__ out) {
  extern __shared__ __attribute__((aligned(16))) char smem[];
  const int tid = threadIdx.x, lane = tid & 63;
  const int w = __builtin_amdgcn_readfirstlane(tid >> 6);
  const u32 lds0 = (u32)(size_t)(LDSP char*)smem;
  const int b = blockIdx.x, xcd = b & 7;
  const int slice = xcd & 3, group = (b >> 3) * 2 + (xcd >> 2);
  const int h = lane >> 5, f = lane & 31;

  const u16* off = (const u16*)(ws + WS_OFF) + lane * OFFP + group * TPG;
  const int o0 = off[w], o1 = off[w + 1];
  const int cnt = o1 - o0;
  int inc = cnt;
#pragma unroll
  for (int o = 1; o < 64; o <<= 1) {
    const int v = __shfl_up(inc, o, 64);
    if (lane >= o) inc += v;
  }
  int* dl = (int*)(smem + L_DL);
  dl[w * 64 + lane] = inc - cnt - o0;
  if (w == 0) ((int*)(smem + L_GS))[lane] = o0;
  if (w == 7) ((int*)(smem + L_GE))[lane] = o1;
  if (lane == 63) ((int*)(smem + L_TT))[w] = inc;
  {
    const u32x4 z = {0, 0, 0, 0};
    *(LDSP u32x4*)(size_t)(lds0 + L_S + w * 2048 + lane * 16) = z;
    *(LDSP u32x4*)(size_t)(lds0 + L_S + w * 2048 + 1024 + lane * 16) = z;
  }

  bf16x8 cw[2], sel[2], one, ident[2];
#pragma unroll
  for (int ct = 0; ct < 2; ++ct)
#pragma unroll
    for (int j = 0; j < 8; ++j)
      cw[ct][j] = (__bf16)(bf16_bits_to_f32(gw[(512 + 8 * h + j) * D + slice * 64 + ct * 32 + f]) * NLOG2E);
#pragma unroll
  for (int s = 0; s < 2; ++s)
#pragma unroll
    for (int j = 0; j < 8; ++j) {
      sel[s][j] = (__bf16)(f == 16 * s + 8 * h + j ? 1.0f : 0.0f);
      ident[s][j] = (__bf16)(f == 16 * s + 8 * (j >> 2) + 4 * h + (j & 3) ? 1.0f : 0.0f);
    }
#pragma unroll
  for (int j = 0; j < 8; ++j) one[j] = (__bf16)((j == 0 && h == 0) ? 1.0f : 0.0f);

  const int tau = w;
  const int tile = group * TPG + tau;
  const int n0 = tile_start(tile), tsz = tile_start(tile + 1) - n0;
  const u32 qb = lds0 + L_QV + w * 8192, vb = qb + 4096;
  {
    const char* pu = ws + WS_PU + (size_t)slice * SLICEB;
#pragma unroll
    for (int j = 0; j < 4; ++j) {
      const int row = 8 * j + (lane >> 3);
      const int node = n0 + (row < tsz ? row : tsz - 1);
      const u32 voff = (u32)node * 256u + (lane & 7) * 16;
      dma16(pu, voff, qb + j * 1024);
      dma16(pu + 128, voff, vb + j * 1024);
    }
  }
  wait_vm0();
  bf16x8 pb[2][2];
  f32x16 agg[2];
  {
    const int blk = (lane >> 4) & 1, qq = (lane & 15) >> 2, p = lane & 3;
#pragma unroll
    for (int ct = 0; ct < 2; ++ct) {
      bf16x8 ub[2];
#pragma unroll
      for (int s = 0; s < 2; ++s) {
        const u32 a = (16 * s + 4 * h + qq) * 128 + (ct * 32 + 16 * blk + 4 * p) * 2;
        pb[ct][s] = join8(lds_read_tr(qb + a), lds_read_tr(qb + a + 8 * 128));
        ub[s] = join8(lds_read_tr(vb + a), lds_read_tr(vb + a + 8 * 128));
      }
      const f32x16 zero = {0};
      agg[ct] = mfma(ident[0], ub[0], zero);
      agg[ct] = mfma(ident[1], ub[1], agg[ct]);
    }
  }
  wg_barrier();

  const int* tt = (const int*)(smem + L_TT);
  int maxt = 0;
#pragma unroll
  for (int j = 0; j < 8; ++j) maxt = max(maxt, tt[j]);
  maxt = __builtin_amdgcn_readfirstlane(maxt);
  const int mytot = __builtin_amdgcn_readfirstlane(tt[tau]);
  const int npass = (maxt + CAPT - 1) / CAPT;

  u32 cq[4];
#pragma unroll
  for (int j = 0; j < 4; ++j) cq[j] = (u32)(((lane & 7) ^ ((4 * j + (lane >> 4)) & 7)) * 16);
  const u32 ce = (u32)(((lane & 1) ^ ((lane >> 4) & 1)) * 16);
  const char* qsrc = ws + WS_QV + (size_t)slice * SLICEB;
  const char* vsrc = qsrc + 128;
  const char* esrc = ws + WS_EP;
  const u32 eb = lds0 + L_E + w * 1024;
  const u32 a1 = lds0 + L_A1 + tau * CAPT * 4 + (lane >> 3) * 16;
  const u32 a2 = lds0 + L_A2 + tau * CAPT * 4 + (lane >> 1) * 4;
  const u32 a3 = lds0 + L_A3 + tau * CAPT * 2 + (lane >> 1) * 2;
  const u32 sb = lds0 + L_S + tau * 2048;
  const int fsw = (f >> 1) & 7;
  const u32 eaddr = eb + f * 32 + ((h ^ ((f >> 3) & 1)) << 4);
  u32 saddr[2], staddr[2][2];
  {
    const int blk = (lane >> 4) & 1, qq = (lane & 15) >> 2, p = lane & 3;
    const int c0 = 16 * blk + 8 * (p & 1) + 4 * (p >> 1);
#pragma unroll
    for (int s = 0; s < 2; ++s) {
      saddr[s] = sb + f * 64 + (((2 * s + h) ^ ((f >> 2) & 3)) << 4);
#pragma unroll
      for (int t = 0; t < 2; ++t) {
        const int nu = 16 * s + 8 * t + 4 * h + qq;
        staddr[s][t] = sb + nu * 64 + (((c0 >> 3) ^ ((nu >> 2) & 3)) << 4) + ((c0 & 7) << 1);
      }
    }
  }

  const u32* grec = (const u32*)(ws + WS_REC);
  for (int pass = 0; pass < npass; ++pass) {
    {
      const int pbk = tid >> 3, j = tid & 7;
      const int gs = ((const int*)(smem + L_GS))[pbk], ge = ((const int*)(smem + L_GE))[pbk];
      u32* A1 = (u32*)(smem + L_A1);
      u32* A2 = (u32*)(smem + L_A2);
      u16* A3 = (u16*)(smem + L_A3);
      for (int i = gs + j; i < ge; i += 8) {
        const u32 rec = grec[pbk * CHUNK + i];
        const int t = rec >> 27;
        const int li = i + dl[t * 64 + pbk] - pass * CAPT;
        if (li >= 0 && li < CAPT) {
          const int r = li & 31, bs = t * CAPT + (li & ~31);
          const int nu = (rec >> 22) & 31;
          const int c = (r & 0x13) | ((r & 4) << 1) | ((r & 8) >> 1);
          A1[bs + (r & 7) * 4 + (r >> 3)] = rec & 0x3FFF00u;
          A2[bs + r] = (u32)(pbk * CHUNK + i) * 32u;
          A3[bs + r] = (u16)(t * 2048 + nu * 64 + (((c >> 3) ^ ((nu >> 2) & 3)) << 4) + ((c & 7) << 1));
        }
      }
      const int tw = ((const int*)(smem + L_TT))[w] - pass * CAPT;
      if (tw > 0 && tw < CAPT && lane < 32) {
        const int li = (tw & ~31) + lane;
        if (li >= tw) {
          const int r = lane, bs = w * CAPT + (tw & ~31);
          A1[bs + (r & 7) * 4 + (r >> 3)] = (u32)N * 256u;
          A2[bs + r] = (u32)E * 32u;
          A3[bs + r] = (u16)(8 * 2048);
        }
      }
    }
    wg_barrier();

    int rem = mytot - pass * CAPT;
    rem = rem < 0 ? 0 : (rem > CAPT ? CAPT : rem);
    const int nsteps = (rem + 31) >> 5;

    auto issue = [&](int t) {
      const u32x4 so = lds_read16u(a1 + t * 128);
      const u32 eo = *(const LDSP u32*)(size_t)(a2 + t * 128);
#pragma unroll
      for (int j = 0; j < 4; ++j) dma16(qsrc, so[j] + cq[j], qb + j * 1024);
#pragma unroll
      for (int j = 0; j < 4; ++j) dma16(vsrc, so[j] + cq[j], vb + j * 1024);
      dma16(esrc, eo + ce, eb);
    };
    if (nsteps > 0) issue(0);
    for (int t = 0; t < nsteps; ++t) {
      wait_vm0();
      const bf16x8 ef = lds_read16(eaddr);
      bf16x8 qf[2][2], vf[2][2];
#pragma unroll
      for (int ct = 0; ct < 2; ++ct)
#pragma unroll
        for (int kk = 0; kk < 2; ++kk) {
          const u32 o = f * 128 + (((4 * ct + 2 * kk + h) ^ fsw) << 4);
          qf[ct][kk] = lds_read16(qb + o);
          vf[ct][kk] = lds_read16(vb + o);
        }
      const u32 sa = *(const LDSP u16*)(size_t)(a3 + t * 64);
      *(LDSP u16*)(size_t)(lds0 + L_S + sa) = (u16)0x3F80;
      mem_fence();
      bf16x8 sf[2], stf[2];
#pragma unroll
      for (int s = 0; s < 2; ++s) {
        sf[s] = lds_read16(saddr[s]);
        stf[s] = join8(lds_read_tr(staddr[s][0]), lds_read_tr(staddr[s][1]));
      }
      mem_fence();
      *(LDSP u16*)(size_t)(lds0 + L_S + sa) = (u16)0;
      wait_lgkm0();
      __builtin_amdgcn_sched_barrier(0);
      if (t + 1 < nsteps) issue(t + 1);
#pragma unroll
      for (int ct = 0; ct < 2; ++ct) {
        const f32x16 zero = {0};
        f32x16 z = mfma(ef, cw[ct], zero);
        z = mfma(stf[0], pb[ct][0], z);
        z = mfma(stf[1], pb[ct][1], z);
        z = mfma(qf[ct][0], sel[0], z);
        z = mfma(qf[ct][1], sel[1], z);
        f32x16 v = mfma(vf[ct][0], sel[0], zero);
        v = mfma(vf[ct][1], sel[1], v);
#pragma unroll
        for (int i = 0; i < 16; ++i) z[i] = __builtin_amdgcn_exp2f(z[i]);
        z = mfma(one, one, z);
        u32x4 m0, m1;
#pragma unroll
        for (int i = 0; i < 4; ++i) {
          m0[i] = pack_bf16(__builtin_amdgcn_rcpf(z[2 * i]) * v[2 * i],
                            __builtin_amdgcn_rcpf(z[2 * i + 1]) * v[2 * i + 1]);
          m1[i] = pack_bf16(__builtin_amdgcn_rcpf(z[8 + 2 * i]) * v[8 + 2 * i],
                            __builtin_amdgcn_rcpf(z[8 + 2 * i + 1]) * v[8 + 2 * i + 1]);
        }
        agg[ct] = mfma(sf[0], __builtin_bit_cast(bf16x8, m0), agg[ct]);
        agg[ct] = mfma(sf[1], __builtin_bit_cast(bf16x8, m1), agg[ct]);
      }
    }
    if (pass + 1 < npass) wg_barrier();
  }

#pragma unroll
  for (int ct = 0; ct < 2; ++ct)
#pragma unroll
    for (int r = 0; r < 16; ++r) {
      const int row = (r & 3) + 8 * (r >> 2) + 4 * h;
      const float a = agg[ct][r];
      const float g = __builtin_amdgcn_rcpf(1.0f + __builtin_amdgcn_exp2f(a * NLOG2E));
      if (row < tsz)
        out[(size_t)(n0 + row) * D + slice * 64 + ct * 32 + f] = __builtin_bit_cast(u16, (__bf16)(a * g));
    }
}

}

extern "C" void kernel_launch(void* const* d_in, const int* in_sizes, int n_in, void* d_out, int out_size,
                              void* d_ws, size_t ws_size, hipStream_t stream) {
  using namespace egc;
  const char* x = (const char*)d_in[0];
  const char* eattr = (const char*)d_in[1];
  const int* src = (const int*)d_in[2];
  const int* dst = (const int*)d_in[3];
  const char* uw = (const char*)d_in[4];
  const char* vw = (const char*)d_in[5];
  const char* gw = (const char*)d_in[6];
  char* ws = (char*)d_ws;
  (void)hipFuncSetAttribute((const void*)prep_kernel, hipFuncAttributeMaxDynamicSharedMemorySize, K1_LDS);
  (void)hipFuncSetAttribute((const void*)edge_kernel, hipFuncAttributeMaxDynamicSharedMemorySize, K2_LDS);
  prep_kernel<<<NPROD + 256, 512, K1_LDS, stream>>>(x, eattr, src, dst, uw, vw, gw, ws);
  edge_kernel<<<256, 512, K2_LDS, stream>>>((const u16*)gw, ws, (u16*)d_out);
}
```

```cpp
#include <hip/hip_runtime.h>
#include <hip/hip_bf16.h>
#include <stdint.h>

namespace egc {

typedef unsigned int u32;
typedef unsigned short u16;
typedef float f32x2 __attribute__((ext_vector_type(2)));
typedef float f32x16 __attribute__((ext_vector_type(16)));
typedef __bf16 bf16x2 __attribute__((ext_vector_type(2)));
typedef __bf16 bf16x4 __attribute__((ext_vector_type(4)));
typedef __bf16 bf16x8 __attribute__((ext_vector_type(8)));
typedef short s16x4 __attribute__((ext_vector_type(4)));
typedef u32 u32x2 __attribute__((ext_vector_type(2)));
typedef u32 u32x4 __attribute__((ext_vector_type(4)));
#define LDSP __attribute__((address_space(3)))

constexpr int N = 10000, D = 256, DE = 16, E = 160000;
constexpr int NPROD = 64;
constexpr int CHUNK = E / NPROD;
constexpr int NTILE = 512;
constexpr int TPG = 8;
constexpr int OFFP = 520;
constexpr int NROWP = 10016;
constexpr size_t SLICEB = (size_t)NROWP * 256;
constexpr int CAPT = 448;
constexpr float NLOG2E = -1.44269504088896340736f;

constexpr size_t WS_QV = 0;
constexpr size_t WS_PU = WS_QV + 4 * SLICEB;
constexpr size_t WS_EP = WS_PU + 4 * SLICEB;
constexpr size_t WS_REC = WS_EP + (size_t)(E + 64) * 32;
constexpr size_t WS_OFF = WS_REC + (size_t)(E + 64) * 4;
constexpr size_t WS_END = WS_OFF + (size_t)NPROD * OFFP * 2;

template <int V>
struct IC {
  static constexpr int value = V;
};

__device__ __forceinline__ int tile_start(int t) { return (625 * t) >> 5; }
__device__ __forceinline__ int tile_of(int d) { return (32 * d + 31) / 625; }

__device__ __forceinline__ f32x16 mfma(bf16x8 a, bf16x8 b, f32x16 c) {
  return __builtin_amdgcn_mfma_f32_32x32x16_bf16(a, b, c, 0, 0, 0);
}
__device__ __forceinline__ u32 pack_bf16(float a, float b) {
  bf16x2 v;
  v[0] = (__bf16)a;
  v[1] = (__bf16)b;
  return __builtin_bit_cast(u32, v);
}
__device__ __forceinline__ float bf16_bits_to_f32(u16 b) { return __builtin_bit_cast(float, (u32)b << 16); }

__device__ __forceinline__ void dma16(const void* sbase, u32 voff, u32 lds) {
  asm volatile("s_mov_b32 m0, %2\n\ts_nop 0\n\tglobal_load_lds_dwordx4 %0, %1"
               :
               : "v"(voff), "s"(sbase), "s"(lds)
               : "memory");
}
typedef __amdgpu_buffer_rsrc_t rsrc_t;
__device__ __forceinline__ rsrc_t make_rsrc(const void* base, u32 bytes) {
  return __builtin_amdgcn_make_buffer_rsrc((void*)base, 0, bytes, 0x00020000);
}
__device__ __forceinline__ void store16_wt(rsrc_t r, u32 off, u32x4 v) {
  __builtin_amdgcn_raw_buffer_store_b128(v, r, off, 0, 16);
}
__device__ __forceinline__ void store4_wt(rsrc_t r, u32 off, u32 v) {
  __builtin_amdgcn_raw_buffer_store_b32(v, r, off, 0, 16);
}
__device__ __forceinline__ void mem_fence() { asm volatile("" ::: "memory"); }
__device__ __forceinline__ void wait_vm0() { asm volatile("s_waitcnt vmcnt(0)" ::: "memory"); }
__device__ __forceinline__ void wait_lgkm0() { asm volatile("s_waitcnt lgkmcnt(0)" ::: "memory"); }
__device__ __forceinline__ void wg_barrier() {
  asm volatile("s_waitcnt lgkmcnt(0)\n\ts_barrier" ::: "memory");
}
__device__ __forceinline__ bf16x8 lds_read16(u32 addr) { return *(const LDSP bf16x8*)(size_t)addr; }
__device__ __forceinline__ u32x4 lds_read16u(u32 addr) { return *(const LDSP u32x4*)(size_t)addr; }
__device__ __forceinline__ s16x4 lds_read_tr(u32 addr) {
  return __builtin_amdgcn_ds_read_tr16_b64_v4i16((LDSP s16x4*)(size_t)addr);
}
__device__ __forceinline__ bf16x8 join8(s16x4 lo, s16x4 hi) {
  u32x2 a = __builtin_bit_cast(u32x2, lo), b = __builtin_bit_cast(u32x2, hi);
  u32x4 r = {a.x, a.y, b.x, b.y};
  return __builtin_bit_cast(bf16x8, r);
}

#define STAMP(wsp, kid, i)
#define CSTAMP(wsp, kid, i, extra)

constexpr int K1_LDS = 163840;
constexpr int NGEMM = 192;

__device__ __forceinline__ u32 wave_scan(u32 v) {
  v += __builtin_amdgcn_update_dpp(0u, v, 0x111, 0xF, 0xF, false);
  v += __builtin_amdgcn_update_dpp(0u, v, 0x112, 0xF, 0xF, false);
  v += __builtin_amdgcn_update_dpp(0u, v, 0x114, 0xF, 0xF, false);
  v += __builtin_amdgcn_update_dpp(0u, v, 0x118, 0xF, 0xF, false);
  v += __builtin_amdgcn_update_dpp(0u, v, 0x142, 0xA, 0xF, false);
  v += __builtin_amdgcn_update_dpp(0u, v, 0x143, 0xC, 0xF, false);
  return v;
}

__device__ __forceinline__ void producer_block(char* smem, int pb, const int* __restrict__ src,
                                               const int* __restrict__ dst, const char* __restrict__ eattr,
                                               char* __restrict__ ws) {
  u32* hist = (u32*)smem;
  u32* wsum = hist + 512;
  u32* srec = wsum + 16;
  u16* seid = (u16*)(srec + 2560);
  constexpr u32 EL = 32768;
  const int tid = threadIdx.x, lane = tid & 63, w = tid >> 6;
  const int e0 = pb * CHUNK;
  STAMP(ws, 0, 0);
  int dd[5], ss[5];
#pragma unroll
  for (int k = 0; k < 5; ++k) {
    const int i = tid + 512 * k;
    const int ii = i < CHUNK ? i : 0;
    dd[k] = dst[e0 + ii];
    ss[k] = src[e0 + ii];
  }
  hist[tid] = 0;
  wg_barrier();
  STAMP(ws, 0, 1);
  u32 rec[5], tl[5], rk[5];
#pragma unroll
  for (int k = 0; k < 5; ++k) {
    const int i = tid + 512 * k;
    const int t = tile_of(dd[k]);
    const u32 dl = dd[k] - tile_start(t);
    rec[k] = ((u32)ss[k] << 8) | (((dl >> 2) & 3) << 22) | (dl << 24) | ((u32)(t & 7) << 29);
    tl[k] = t;
    rk[k] = 0;
    if (i < CHUNK) rk[k] = atomicAdd(&hist[t], 1u);
  }
  {
    const char* ein = eattr + (size_t)e0 * 32;
    const u32 lds0 = (u32)(size_t)(LDSP char*)smem;
    const int wu = __builtin_amdgcn_readfirstlane(w);
#pragma unroll
    for (int k = 0; k < 10; ++k) {
      const int i = tid + 512 * k;
      dma16(ein, (u32)(i < 2 * CHUNK ? i : 0) * 16u, lds0 + EL + (wu * 64 + 512 * k) * 16);
    }
  }
  wg_barrier();
  const u32 c = hist[tid];
  const u32 inc = wave_scan(c);
  if (lane == 63) wsum[w] = inc;
  wg_barrier();
  u32 wb = 0;
#pragma unroll
  for (int j = 0; j < 8; ++j) wb += j < w ? wsum[j] : 0u;
  const u32 base = wb + inc - c;
  hist[tid] = base;
  u16* off = (u16*)(ws + WS_OFF) + pb * OFFP;
  off[tid] = (u16)base;
  if (tid < OFFP - 512) off[512 + tid] = (u16)CHUNK;
  wg_barrier();
#pragma unroll
  for (int k = 0; k < 5; ++k) {
    const int i = tid + 512 * k;
    if (i < CHUNK) {
      const u32 p = hist[tl[k]] + rk[k];
      srec[p] = rec[k];
      seid[p] = (u16)i;
    }
  }
  wait_vm0();
  wg_barrier();
  STAMP(ws, 0, 2);
  const rsrc_t wsr = make_rsrc(ws, (u32)WS_END);
#pragma unroll
  for (int k = 0; k < 10; ++k) {
    const int i = tid + 512 * k;
    if (i < 2 * CHUNK)
      store16_wt(wsr, (u32)WS_EP + (u32)e0 * 32u + i * 16,
                 *(const u32x4*)(smem + EL + (int)seid[i >> 1] * 32 + (i & 1) * 16));
  }
  if (tid < CHUNK / 4) store16_wt(wsr, (u32)WS_REC + (u32)e0 * 4u + tid * 16, *(const u32x4*)(srec + tid * 4));
  if (tid + 512 < CHUNK / 4)
    store16_wt(wsr, (u32)WS_REC + (u32)e0 * 4u + (tid + 512) * 16, *(const u32x4*)(srec + (tid + 512) * 4));
  STAMP(ws, 0, 3);
  if (pb == 0) {
    const u32x4 z = {0, 0, 0, 0};
    if (tid < 64) *(u32x4*)(ws + WS_QV + (tid >> 4) * SLICEB + (size_t)N * 256 + (tid & 15) * 16) = z;
    if (tid >= 64 && tid < 66) *(u32x4*)(ws + WS_EP + (size_t)E * 32 + (tid - 64) * 16) = z;
  }
}

__device__ __forceinline__ void gemm_block(char* smem, int gb, const char* __restrict__ x,
                                           const char* __restrict__ uw, const char* __restrict__ vw,
                                           const char* __restrict__ gw, char* __restrict__ ws) {
  const int tid = threadIdx.x, lane = tid & 63;
  const int w = __builtin_amdgcn_readfirstlane(tid >> 6);
  const u32 lds0 = (u32)(size_t)(LDSP char*)smem;
  const int q = gb >> 3, xcd = gb & 7;
  const int cg = q / 3;
  const int type = cg & 3, half = cg >> 2;
  const int rp0 = (q - 3 * cg) * 8 + xcd;
  const int cw4 = w & 3, rg = w >> 2;
  const char* W = (type == 0 ? gw + 256 * 512 : type == 1 ? vw : type == 2 ? gw : uw) + half * 256;
  const float scale = (type & 1) ? 1.0f : NLOG2E;
  const int ctg = half * 4 + cw4;
  const u32 dest = (u32)(type < 2 ? WS_QV : WS_PU) + (type & 1) * 128 + (u32)(ctg >> 1) * (u32)SLICEB;
  const rsrc_t wsr = make_rsrc(ws, (u32)WS_END);

  constexpr u32 TB = 131072;
  const bool loader = (w & 1) == 0;
  auto load_x1 = [&](int job, int i) {
    const u32 xb = ((job + 2) & 3) * 32768 + rg * 16384;
    const int j = (cw4 >> 1) * 8 + i;
    const int row = 2 * j + (lane >> 5);
    int node = (rp0 + 24 * job) * 64 + rg * 32 + row;
    node = node < N ? node : N - 1;
    const u32 voff = (u32)node * 512u + (u32)(((lane & 31) ^ (row & 15)) * 16);
    dma16(x, voff, lds0 + xb + j * 1024);
  };
  auto load_x = [&](int job) {
    if (loader) {
#pragma unroll
      for (int i = 0; i < 8; ++i) load_x1(job, i);
    }
  };
  STAMP(ws, 0, 0);
#pragma unroll
  for (int i = 0; i < 8; ++i) {
    const int j = w * 8 + i;
    const int k = 4 * j + (lane >> 4);
    const u32 voff = (u32)k * 512u + (u32)(((lane & 15) ^ ((k & 3) << 2)) * 16);
    dma16(W, voff, lds0 + j * 1024);
  }
  load_x(0);
  if (loader) asm volatile("s_waitcnt vmcnt(8)" ::: "memory"); else wait_vm0();
  wg_barrier();
  load_x(1);
  STAMP(ws, 0, 1);
  bf16x8 wf[16];
  const int h = lane >> 5, blk = (lane >> 4) & 1, qq = (lane & 15) >> 2, p = lane & 3;
  {
    const int col0 = cw4 * 32 + 16 * (p & 1) + 8 * blk + 4 * (p >> 1);
    const u32 a0 = lds0 + (8 * h + qq) * 256 + ((((col0 >> 3) ^ (qq << 2)) << 4) | ((col0 & 7) << 1));
#pragma unroll
    for (int ks = 0; ks < 16; ++ks)
      wf[ks] = join8(lds_read_tr(a0 + ks * 4096), lds_read_tr(a0 + ks * 4096 + 1024));
  }
  asm volatile("s_waitcnt vmcnt(8)" ::: "memory");
  wg_barrier();
  load_x(2);
  STAMP(ws, 0, 2);

  const int n = lane & 31;
  u32 xaddr[8];
#pragma unroll
  for (int c = 0; c < 8; ++c) xaddr[c] = lds0 + rg * 16384 + n * 512 + (((2 * c + h) ^ (n & 15)) << 4);
  auto xfrag = [&](int job, int ks) { return lds_read16(xaddr[ks & 7] + ((job + 2) & 3) * 32768 + (ks >> 3) * 256); };
  const u32 tb = lds0 + TB + (w >> 1) * 8192;
  const int cpar = w & 1;
  const u32 tput = tb + n * 128, tsw = (4 * cpar + 2 * h) ^ (n & 7);
  const u32 tget = tb + (lane >> 3) * 128 + (((lane & 7) ^ (lane >> 3)) << 4);
  f32x16 accs[2];
  bf16x8 xf[8];
#pragma unroll
  for (int ks = 0; ks < 4; ++ks) xf[ks] = xfrag(0, ks);

  auto job_body = [&](auto jc) {
    constexpr int job = decltype(jc)::value;
    f32x16& acc = accs[job & 1];
    const f32x16& accp = accs[(job + 1) & 1];
    u32x4 c0, c1, o[4];
    if (loader) {
      if (job < 5) asm volatile("s_waitcnt vmcnt(8)" ::: "memory");
      if (job == 5) wait_vm0();
    }
    wg_barrier();
    auto gap = [&](auto kc) {
      constexpr int ks = decltype(kc)::value;
      if (job < 7) {
        if (ks == 0) {
          const f32x16 zero = {0};
          acc = mfma(wf[0], xf[0], zero);
        } else {
          acc = mfma(wf[ks], xf[ks & 7], acc);
        }
        asm volatile("" : "+v"(acc));
        __builtin_amdgcn_sched_barrier(0);
        if (ks < 12) xf[(ks + 4) & 7] = xfrag(job, ks + 4);
        else if (job < 6) xf[(ks + 4) & 7] = xfrag(job + 1, ks - 12);
      }
      if (job >= 1 && job <= 7) {
        if (ks >= 1 && ks <= 4) {
          constexpr int i = ks - 1;
          c0[i] = pack_bf16(accp[2 * i] * scale, accp[2 * i + 1] * scale);
          c1[i] = pack_bf16(accp[8 + 2 * i] * scale, accp[8 + 2 * i + 1] * scale);
        }
        if (ks == 5) {
          const u32 t = tput + ((job - 1) & 1) * 4096;
          *(LDSP u32x4*)(size_t)(t + (tsw << 4)) = c0;
          *(LDSP u32x4*)(size_t)(t + ((tsw ^ 1) << 4)) = c1;
        }
      }
      if (job >= 2 && !loader) {
        if (ks == 0) {
#pragma unroll
          for (int i = 0; i < 4; ++i) o[i] = lds_read16u(tget + ((job - 2) & 1) * 4096 + i * 1024);
        }
        if (ks >= 6 && ks <= 9) {
          constexpr int i = (ks - 6) & 3;
          const int rp = rp0 + 24 * (job - 2);
          if (job - 2 < 6 || rp * 64 < N) {
            const int nn = 8 * i + (lane >> 3);
            int node = rp * 64 + rg * 32 + nn;
            node = node < N ? node : N + 1 + (nn & 7);
            store16_wt(wsr, dest + (u32)node * 256u + (lane & 7) * 16, o[i]);
          }
        }
      }
      if (job <= 3 && ks >= 6 && ks <= 13 && loader) load_x1(job + 3, ks - 6);
      __builtin_amdgcn_sched_barrier(0);
    };
    gap(IC<0>{}); gap(IC<1>{}); gap(IC<2>{}); gap(IC<3>{}); gap(IC<4>{}); gap(IC<5>{}); gap(IC<6>{}); gap(IC<7>{});
    gap(IC<8>{}); gap(IC<9>{}); gap(IC<10>{}); gap(IC<11>{}); gap(IC<12>{}); gap(IC<13>{}); gap(IC<14>{});
    gap(IC<15>{});
    STAMP(ws, 0, 3 + (job < 7 ? job : 6));
  };
  job_body(IC<0>{}); job_body(IC<1>{}); job_body(IC<2>{}); job_body(IC<3>{}); job_body(IC<4>{});
  job_body(IC<5>{}); job_body(IC<6>{}); job_body(IC<7>{}); job_body(IC<8>{});
}

__global__ void __launch_bounds__(512, 2)
prep_kernel(const char* __restrict__ x, const char* __restrict__ eattr, const int* __restrict__ src,
            const int* __restrict__ dst, const char* __restrict__ uw, const char* __restrict__ vw,
            const char* __restrict__ gw, char* __restrict__ ws) {
  extern __shared__ __attribute__((aligned(16))) char smem[];
  const int b = blockIdx.x;
  if (b < NPROD)
    producer_block(smem, b, src, dst, eattr, ws);
  else
    gemm_block(smem, b - NPROD, x, uw, vw, gw, ws);
}

constexpr u32 L_QV = 0;
constexpr u32 L_E = 65536;
constexpr u32 L_S = L_E + 8 * 4096;
constexpr u32 L_A1 = L_S + 8 * 2048 + 128;
constexpr u32 L_A2 = L_A1 + TPG * CAPT * 4;
constexpr u32 L_DL = L_A2 + TPG * CAPT * 8;
constexpr u32 L_GS = L_DL + 8 * 64 * 4;
constexpr u32 L_GE = L_GS + 256;
constexpr u32 L_TT = L_GE + 256;
constexpr u32 K2_LDS = L_TT + 64;
static_assert(K2_LDS <= 163840, "LDS budget");

#define SB() __builtin_amdgcn_sched_barrier(0)
__device__ __forceinline__ void mfma_acc(f32x16& c, bf16x8 a, bf16x8 b) {
  c = mfma(a, b, c);
  asm volatile("" : "+v"(c));
  SB();
}
__device__ __forceinline__ void mfma_new(f32x16& c, bf16x8 a, bf16x8 b) {
  const f32x16 zero = {0};
  c = mfma(a, b, zero);
  asm volatile("" : "+v"(c));
  SB();
}
__device__ __forceinline__ void arrived4(const bf16x8 (&v)[4]) {
  asm volatile("" ::"v"(v[0]), "v"(v[1]), "v"(v[2]), "v"(v[3]) : "memory");
}
__device__ __forceinline__ void dma16x4(const void* sbase, const u32 (&vo)[4], u32 lds) {
  asm volatile(
      "s_mov_b32 m0, %5\n\ts_nop 0\n\t"
      "global_load_lds_dwordx4 %0, %4\n\t"
      "global_load_lds_dwordx4 %1, %4 offset:1024\n\t"
      "global_load_lds_dwordx4 %2, %4 offset:2048\n\t"
      "global_load_lds_dwordx4 %3, %4 offset:3072"
      :
      : "v"(vo[0]), "v"(vo[1]), "v"(vo[2]), "v"(vo[3]), "s"(sbase), "s"(lds)
      : "memory");
}
template <int A, int B>
__device__ __forceinline__ void exp_range(f32x16& d, const f32x16& s) {
#pragma unroll
  for (int i = A; i < B; ++i) d[i] = __builtin_amdgcn_exp2f(s[i]);
}
template <int A, int B>
__device__ __forceinline__ void rcp_range(f32x16& d) {
#pragma unroll
  for (int i = A; i < B; ++i) d[i] = __builtin_amdgcn_rcpf(d[i]);
}
__device__ __forceinline__ void mul_pack(u32x4& m0, u32x4& m1, const f32x16& g, const f32x16& v) {
#pragma unroll
  for (int i = 0; i < 4; ++i) {
    const f32x2 a = (f32x2){g[2 * i], g[2 * i + 1]} * (f32x2){v[2 * i], v[2 * i + 1]};
    const f32x2 c = (f32x2){g[8 + 2 * i], g[8 + 2 * i + 1]} * (f32x2){v[8 + 2 * i], v[8 + 2 * i + 1]};
    m0[i] = pack_bf16(a.x, a.y);
    m1[i] = pack_bf16(c.x, c.y);
  }
}
template <int NV>
__device__ __forceinline__ void wait_vm() { asm volatile("s_waitcnt vmcnt(%0)" ::"n"(NV) : "memory"); }
template <int NV>
__device__ __forceinline__ void wait_lgkm() { asm volatile("s_waitcnt lgkmcnt(%0)" ::"n"(NV) : "memory"); }

__global__ void __launch_bounds__(512, 2)
edge_kernel(const u16* __restrict__ gw, const char* __restrict__ ws, u16* __restrict__ out) {
  extern __shared__ __attribute__((aligned(16))) char smem[];
  const int tid = threadIdx.x, lane = tid & 63;
  const int w = __builtin_amdgcn_readfirstlane(tid >> 6);
  const u32 lds0 = (u32)(size_t)(LDSP char*)smem;
  const int b = blockIdx.x, xcd = b & 7;
  const int slice = xcd & 3, group = (b >> 3) * 2 + (xcd >> 2);
  const int h = lane >> 5, f = lane & 31;
  STAMP(ws, 1, 0);

  const u16* off = (const u16*)(ws + WS_OFF) + lane * OFFP + group * TPG;
  const int o0 = off[w], o1 = off[w + 1];
  const int tau = w;
  const int tile = group * TPG + tau;
  const int n0 = tile_start(tile), tsz = tile_start(tile + 1) - n0;
  const u32 qb = lds0 + L_QV + w * 8192, vb = qb + 4096;
  u16 cwraw[2][8];
#pragma unroll
  for (int ct = 0; ct < 2; ++ct)
#pragma unroll
    for (int j = 0; j < 8; ++j) cwraw[ct][j] = gw[(512 + 8 * h + j) * D + slice * 64 + ct * 32 + f];
  {
    const char* pu = ws + WS_PU + (size_t)slice * SLICEB;
#pragma unroll
    for (int j = 0; j < 4; ++j) {
      const int row = 8 * j + (lane >> 3);
      const int node = n0 + (row < tsz ? row : tsz - 1);
      const u32 voff = (u32)node * 256u + (lane & 7) * 16;
      dma16(pu, voff, qb + j * 1024);
      dma16(pu + 128, voff, vb + j * 1024);
    }
  }
  {
    const u32x4 z = {0, 0, 0, 0};
    *(LDSP u32x4*)(size_t)(lds0 + L_S + w * 2048 + lane * 16) = z;
    *(LDSP u32x4*)(size_t)(lds0 + L_S + w * 2048 + 1024 + lane * 16) = z;
  }
  const int cnt = o1 - o0;
  const int inc = (int)wave_scan((u32)cnt);
  int* dl = (int*)(smem + L_DL);
  dl[w * 64 + lane] = inc - cnt - o0;
  if (w == 0) ((int*)(smem + L_GS))[lane] = o0;
  if (w == 7) ((int*)(smem + L_GE))[lane] = o1;
  if (lane == 63) ((int*)(smem + L_TT))[w] = inc;
  STAMP(ws, 1, 1);
  wg_barrier();
  STAMP(ws, 1, 2);

  const u32* grec = (const u32*)(ws + WS_REC);
  const int pbk = tid >> 3, pj = tid & 7;
  const int gs = ((const int*)(smem + L_GS))[pbk], ge = ((const int*)(smem + L_GE))[pbk];
  u32 rec0[8];
#pragma unroll
  for (int k = 0; k < 8; ++k) {
    const int i = gs + pj + 8 * k;
    rec0[k] = grec[pbk * CHUNK + (i < ge ? i : 0)];
  }

  const int* tt = (const int*)(smem + L_TT);
  int maxt = 0;
#pragma unroll
  for (int j = 0; j < 8; ++j) maxt = max(maxt, tt[j]);
  maxt = __builtin_amdgcn_readfirstlane(maxt);
  const int mytot = __builtin_amdgcn_readfirstlane(tt[tau]);
  const int npass = (maxt + CAPT - 1) / CAPT;

  bf16x8 cw[2], sel[2], one;
#pragma unroll
  for (int ct = 0; ct < 2; ++ct)
#pragma unroll
    for (int j = 0; j < 8; ++j) cw[ct][j] = (__bf16)(bf16_bits_to_f32(cwraw[ct][j]) * NLOG2E);
#pragma unroll
  for (int s = 0; s < 2; ++s)
#pragma unroll
    for (int j = 0; j < 8; ++j) sel[s][j] = (__bf16)(f == 16 * s + 8 * h + j ? 1.0f : 0.0f);
#pragma unroll
  for (int j = 0; j < 8; ++j) one[j] = (__bf16)((j == 0 && h == 0) ? 1.0f : 0.0f);

  bf16x8 pb[2][2];
  f32x16 agg[2];
  wait_vm<8>();
  {
    bf16x8 ident[2];
#pragma unroll
    for (int s = 0; s < 2; ++s)
#pragma unroll
      for (int j = 0; j < 8; ++j)
        ident[s][j] = (__bf16)(f == 16 * s + 8 * (j >> 2) + 4 * h + (j & 3) ? 1.0f : 0.0f);
    const int blk = (lane >> 4) & 1, qq = (lane & 15) >> 2, p = lane & 3;
#pragma unroll
    for (int ct = 0; ct < 2; ++ct) {
      bf16x8 ub[2];
#pragma unroll
      for (int s = 0; s < 2; ++s) {
        const u32 a = (16 * s + 4 * h + qq) * 128 + (ct * 32 + 16 * blk + 4 * p) * 2;
        pb[ct][s] = join8(lds_read_tr(qb + a), lds_read_tr(qb + a + 8 * 128));
        ub[s] = join8(lds_read_tr(vb + a), lds_read_tr(vb + a + 8 * 128));
      }
      const f32x16 zero = {0};
      agg[ct] = mfma(ident[0], ub[0], zero);
      agg[ct] = mfma(ident[1], ub[1], agg[ct]);
    }
  }

  u32 cq[4];
#pragma unroll
  for (int j = 0; j < 4; ++j) cq[j] = (u32)(((lane & 7) ^ (4 * (j & 1) + (lane >> 4))) * 16 + 3072 - 1024 * j);
  const u32 ce = (u32)(((lane & 1) ^ ((lane >> 4) & 1)) * 16);
  const char* qsrc = ws + WS_QV + (size_t)slice * SLICEB - 3072;
  const char* vsrc = qsrc + 128;
  const char* esrc = ws + WS_EP;
  const u32 eb = lds0 + L_E + w * 4096;
  const u32 a1 = lds0 + L_A1 + tau * CAPT * 4 + (lane >> 3) * 16;
  const u32 a2 = lds0 + L_A2 + tau * CAPT * 8 + (lane >> 1) * 8;
  const u32 sb = lds0 + L_S + tau * 2048;
  const u32 simg = lds0 + L_S;
  const int sr = (f & 0x13) | ((f & 4) << 1) | ((f & 8) >> 1);
  const int sq = ((sr & 7) << 2) | (sr >> 3);
  const int fsw = (sr >> 1) & 7;
  const u32 eaddr = eb + sq * 32 + ((h ^ ((sq >> 3) & 1)) << 4);
  u32 qaddr[4];
#pragma unroll
  for (int c = 0; c < 4; ++c) qaddr[c] = qb + sr * 128 + (((2 * c + h) ^ fsw) << 4);
  u32 saddr[2], staddr[2];
  {
    const int blk = (lane >> 4) & 1, qq = (lane & 15) >> 2, p = lane & 3;
    const int c0 = 16 * blk + 8 * (p & 1) + 4 * (p >> 1);
#pragma unroll
    for (int s = 0; s < 2; ++s) {
      saddr[s] = sb + f * 64 + (((2 * s + h) ^ ((f >> 2) & 3)) << 4);
      const int nu = 8 * s + 4 * h + qq;
      staddr[s] = sb + nu * 64 + (((c0 >> 3) ^ ((nu >> 2) & 3)) << 4) + ((c0 & 7) << 1);
    }
  }
  auto read_st = [&](int s) { return join8(lds_read_tr(staddr[0] + s * 1024), lds_read_tr(staddr[1] + s * 1024)); };
  STAMP(ws, 1, 3);

  auto build_lists = [&](int pass, bool first) {
    u32* A1 = (u32*)(smem + L_A1);
    u32x2* A2 = (u32x2*)(smem + L_A2);
    const int tb = threadIdx.x >> 3, tj = threadIdx.x & 7;
    const int bs0 = ((const int*)(smem + L_GS))[tb], be0 = ((const int*)(smem + L_GE))[tb];
    const int* dlb = dl + tb;
    const int pos0 = tb * CHUNK;
    auto put = [&](u32 rec, int i) {
      const int t = rec >> 29;
      const u32 li = (u32)(i + dlb[t * 64] - pass * CAPT);
      if (li < (u32)CAPT) {
        const u32 idx = t * CAPT + li;
        A1[idx] = rec & 0x3FFF00u;
        const u32 sa = ((rec >> 18) & 0x3FF0u) ^ (((li & 3) << 4) | ((li >> 1) & 0xE));
        A2[idx] = (u32x2){(u32)(pos0 + i) << 5, sa};
      }
    };
    int i0 = bs0 + tj;
    if (first) {
#pragma unroll
      for (int k = 0; k < 8; ++k) {
        const int i = bs0 + tj + 8 * k;
        if (i < be0) put(rec0[k], i);
      }
      i0 += 64;
    }
    for (int i = i0; i < be0; i += 8) put(grec[pos0 + i], i);
    const int tw = ((const int*)(smem + L_TT))[w] - pass * CAPT;
    const int ln = threadIdx.x & 63;
    if (tw > 0 && tw < CAPT && ln < 32) {
      const int li = (tw & ~31) + ln;
      if (li >= tw) {
        A1[w * CAPT + li] = (u32)N * 256u;
        A2[w * CAPT + li] = (u32x2){(u32)E * 32u, 8u * 2048u};
      }
    }
  };
  if (npass > 0) build_lists(0, true);

  for (int pass = 0; pass < npass; ++pass) {
    if (pass > 0) build_lists(pass, false);
    STAMP(ws, 1, 4);
    wait_vm0();
    wg_barrier();
    STAMP(ws, 1, 5);

    int rem = mytot - pass * CAPT;
    rem = rem < 0 ? 0 : (rem > CAPT ? CAPT : rem);
    const int nsteps = __builtin_amdgcn_readfirstlane((rem + 31) >> 5);
    if (nsteps > 0) {
      const int last = nsteps - 1;
      auto clampt = [&](int t) { return t < last ? t : last; };
      auto rd16 = [&](int t) -> u32 { return *(const LDSP u32*)(size_t)(a2 + t * 256 + 4); };
      auto rd32 = [&](int t) -> u32 { return *(const LDSP u32*)(size_t)(a2 + t * 256); };
      auto s_write = [&](u32 a, u16 v) { *(LDSP u16*)(size_t)(simg + a) = v; };
      auto dma_e = [&](u32 eo, int t) { dma16(esrc, eo + ce, eb + (t & 3) * 1024); };
      u32 vo[4];
      auto set_vo = [&](const u32x4& so) {
#pragma unroll
        for (int j = 0; j < 4; ++j) vo[j] = so[j] + cq[j];
      };
      const f32x16 zero = {0};

      set_vo(lds_read16u(a1));
      dma_e(rd32(0), 0);
      dma16x4(qsrc, vo, qb);
      dma16x4(vsrc, vo, vb);
      dma_e(rd32(clampt(1)), 1);
      dma_e(rd32(clampt(2)), 2);
      u32 sa = rd16(0);
      u32 sa1 = rd16(clampt(1));
      s_write(sa, (u16)0x3F80);
      mem_fence();
      bf16x8 stf[2], sf[2], qf[4], vfr[4], ef;
#pragma unroll
      for (int s = 0; s < 2; ++s) stf[s] = read_st(s);
      const u32x4 so1 = lds_read16u(a1 + clampt(1) * 128);
      const u32 eo3 = rd32(clampt(3));
      wait_vm<6>();
      ef = lds_read16(eaddr);
#pragma unroll
      for (int c = 0; c < 4; ++c) qf[c] = lds_read16(qaddr[c]);
      set_vo(so1);
      arrived4(qf);
      dma16x4(qsrc, vo, qb);
      f32x16 za, zb, ea, ebv, vf;
      za = mfma(ef, cw[0], zero);
      zb = mfma(ef, cw[1], zero);
      za = mfma(stf[0], pb[0][0], za);
      zb = mfma(stf[0], pb[1][0], zb);
      za = mfma(stf[1], pb[0][1], za);
      zb = mfma(stf[1], pb[1][1], zb);
      za = mfma(qf[0], sel[0], za);
      zb = mfma(qf[2], sel[0], zb);
      za = mfma(qf[1], sel[1], za);
      zb = mfma(qf[3], sel[1], zb);
      u32x4 m0 = {0, 0, 0, 0}, m1 = {0, 0, 0, 0};
      sf[0] = __builtin_bit_cast(bf16x8, m0);
      sf[1] = sf[0];
      dma_e(eo3, 3);
      SB();

      CSTAMP(ws, 1, 9, 0);
      STAMP(ws, 1, 11);
      for (int t = 0; t < nsteps; ++t) {
        wait_vm<5>();
#pragma unroll
        for (int c = 0; c < 4; ++c) vfr[c] = lds_read16(qaddr[c] + 4096);
        SB();
        const u32x4 so = lds_read16u(a1 + clampt(t + 2) * 128);
        const u32 eo = rd32(clampt(t + 4));
        const u32 sa2 = rd16(clampt(t + 2));
        mfma_acc(agg[1], sf[0], __builtin_bit_cast(bf16x8, m0));
        exp_range<0, 4>(ea, za);
        SB();
        mfma_acc(agg[1], sf[1], __builtin_bit_cast(bf16x8, m1));
        SB();
        sf[0] = lds_read16(saddr[0]);
        sf[1] = lds_read16(saddr[1]);
        mem_fence();
        s_write(sa, (u16)0);
        s_write(sa1, (u16)0x3F80);
        mem_fence();
        stf[0] = read_st(0);
        stf[1] = read_st(1);
        exp_range<4, 8>(ea, za);
        SB();
        mfma_new(vf, vfr[0], sel[0]);
        ef = lds_read16(eaddr + ((t + 1) & 3) * 1024);
        exp_range<8, 12>(ea, za);
        SB();
        mfma_acc(vf, vfr[1], sel[1]);
        arrived4(vfr);
        dma16x4(vsrc, vo, vb);
        sa = sa1;
        sa1 = sa2;
        exp_range<12, 16>(ea, za);
        SB();
        set_vo(so);
        exp_range<0, 2>(ebv, zb);
        SB();
        mfma_acc(ea, one, one);
        exp_range<2, 6>(ebv, zb);
        SB();
        mfma_new(za, ef, cw[0]);
        exp_range<6, 10>(ebv, zb);
        SB();
        mfma_acc(za, stf[0], pb[0][0]);
        exp_range<10, 13>(ebv, zb);
        SB();
        mfma_acc(za, stf[1], pb[0][1]);
        wait_vm<5>();
#pragma unroll
        for (int c = 0; c < 4; ++c) qf[c] = lds_read16(qaddr[c]);
        exp_range<13, 16>(ebv, zb);
        SB();
        rcp_range<0, 3>(ea);
        SB();
        mfma_acc(ebv, one, one);
        rcp_range<3, 6>(ea);
        SB();
        mfma_acc(za, qf[0], sel[0]);
        rcp_range<6, 9>(ea);
        SB();
        mfma_acc(za, qf[1], sel[1]);
        arrived4(qf);
        dma16x4(qsrc, vo, qb);
        dma_e(eo, t + 4);
        rcp_range<9, 11>(ea);
        SB();
        mfma_new(zb, ef, cw[1]);
        rcp_range<11, 13>(ea);
        SB();
        mfma_acc(zb, stf[0], pb[1][0]);
        rcp_range<13, 14>(ea);
        SB();
        mfma_acc(zb, stf[1], pb[1][1]);
        rcp_range<14, 16>(ea);
        SB();
        mul_pack(m0, m1, ea, vf);
        SB();
        mfma_new(vf, vfr[2], sel[0]);
        rcp_range<0, 3>(ebv);
        SB();
        mfma_acc(vf, vfr[3], sel[1]);
        rcp_range<3, 6>(ebv);
        SB();
        mfma_acc(agg[0], sf[0], __builtin_bit_cast(bf16x8, m0));
        rcp_range<6, 9>(ebv);
        SB();
        mfma_acc(agg[0], sf[1], __builtin_bit_cast(bf16x8, m1));
        rcp_range<9, 11>(ebv);
        SB();
        mfma_acc(zb, qf[2], sel[0]);
        rcp_range<11, 13>(ebv);
        SB();
        mfma_acc(zb, qf[3], sel[1]);
        rcp_range<13, 16>(ebv);
        SB();
        mul_pack(m0, m1, ebv, vf);
        SB();
      }
      CSTAMP(ws, 1, 10, nsteps);
      STAMP(ws, 1, 12);
      agg[1] = mfma(sf[0], __builtin_bit_cast(bf16x8, m0), agg[1]);
      agg[1] = mfma(sf[1], __builtin_bit_cast(bf16x8, m1), agg[1]);
      s_write(sa, (u16)0);
      wait_vm0();
    }
    if (pass + 1 < npass) wg_barrier();
  }
  STAMP(ws, 1, 6);

#pragma unroll
  for (int ct = 0; ct < 2; ++ct)
#pragma unroll
    for (int r = 0; r < 16; ++r) {
      const int row = (r & 3) + 8 * (r >> 2) + 4 * h;
      const float a = agg[ct][r];
      const float g = __builtin_amdgcn_rcpf(1.0f + __builtin_amdgcn_exp2f(a * NLOG2E));
      if (row < tsz)
        out[(size_t)(n0 + row) * D + slice * 64 + ct * 32 + f] = __builtin_bit_cast(u16, (__bf16)(a * g));
    }
  STAMP(ws, 1, 7);
  STAMP(ws, 1, 8);
}

inline void launch_k1(void* const* d_in, void* d_ws, hipStream_t stream) {
  (void)hipFuncSetAttribute((const void*)prep_kernel, hipFuncAttributeMaxDynamicSharedMemorySize, K1_LDS);
  prep_kernel<<<NPROD + NGEMM, 512, K1_LDS, stream>>>((const char*)d_in[0], (const char*)d_in[1], (const int*)d_in[2],
                                                    (const int*)d_in[3], (const char*)d_in[4], (const char*)d_in[5],
                                                    (const char*)d_in[6], (char*)d_ws);
}
inline void launch_k2(void* const* d_in, void* d_out, void* d_ws, hipStream_t stream) {
  (void)hipFuncSetAttribute((const void*)edge_kernel, hipFuncAttributeMaxDynamicSharedMemorySize, K2_LDS);
  edge_kernel<<<256, 512, K2_LDS, stream>>>((const u16*)d_in[6], (const char*)d_ws, (u16*)d_out);
}

}

extern "C" void kernel_launch(void* const* d_in, const int* in_sizes, int n_in, void* d_out, int out_size,
                              void* d_ws, size_t ws_size, hipStream_t stream) {
  egc::launch_k1(d_in, d_ws, stream);
  egc::launch_k2(d_in, d_out, d_ws, stream);
}
```

```cpp
#include <hip/hip_runtime.h>
#include <hip/hip_bf16.h>
#include <stdint.h>

namespace egc {

typedef unsigned int u32;
typedef unsigned short u16;
typedef float f32x2 __attribute__((ext_vector_type(2)));
typedef float f32x16 __attribute__((ext_vector_type(16)));
typedef __bf16 bf16x2 __attribute__((ext_vector_type(2)));
typedef __bf16 bf16x4 __attribute__((ext_vector_type(4)));
typedef __bf16 bf16x8 __attribute__((ext_vector_type(8)));
typedef short s16x4 __attribute__((ext_vector_type(4)));
typedef u32 u32x2 __attribute__((ext_vector_type(2)));
typedef u32 u32x4 __attribute__((ext_vector_type(4)));
#define LDSP __attribute__((address_space(3)))

constexpr int N = 10000, D = 256, DE = 16, E = 160000;
constexpr int NPROD = 64;
constexpr int CHUNK = E / NPROD;
constexpr int NTILE = 512;
constexpr int TPG = 8;
constexpr int OFFP = 520;
constexpr int NROWP = 10016;
constexpr size_t SLICEB = (size_t)NROWP * 256;
constexpr int CAPT = 448;
constexpr float NLOG2E = -1.44269504088896340736f;

constexpr size_t WS_QV = 0;
constexpr size_t WS_PU = WS_QV + 4 * SLICEB;
constexpr size_t WS_EP = WS_PU + 4 * SLICEB;
constexpr size_t WS_REC = WS_EP + (size_t)(E + 64) * 32;
constexpr size_t WS_OFF = WS_REC + (size_t)(E + 64) * 4;
constexpr int NGROUP = NTILE / TPG;
constexpr int CAPG = 64;
constexpr size_t WS_SLOT = (WS_OFF + (size_t)NPROD * OFFP * 2 + 255) & ~(size_t)255;
constexpr size_t WS_CWF = WS_SLOT + (size_t)NGROUP * NPROD * CAPG * 4;
constexpr size_t WS_END = WS_CWF + 4 * 2 * 64 * 16;

template <int V>
struct IC {
  static constexpr int value = V;
};

__device__ __forceinline__ int tile_start(int t) { return (625 * t) >> 5; }
__device__ __forceinline__ int tile_of(int d) { return (32 * d + 31) / 625; }

__device__ __forceinline__ f32x16 mfma(bf16x8 a, bf16x8 b, f32x16 c) {
  return __builtin_amdgcn_mfma_f32_32x32x16_bf16(a, b, c, 0, 0, 0);
}
__device__ __forceinline__ u32 pack_bf16(float a, float b) {
  bf16x2 v;
  v[0] = (__bf16)a;
  v[1] = (__bf16)b;
  return __builtin_bit_cast(u32, v);
}
__device__ __forceinline__ float bf16_bits_to_f32(u16 b) { return __builtin_bit_cast(float, (u32)b << 16); }

__device__ __forceinline__ void dma16(const void* sbase, u32 voff, u32 lds) {
  asm volatile("s_mov_b32 m0, %2\n\ts_nop 0\n\tglobal_load_lds_dwordx4 %0, %1"
               :
               : "v"(voff), "s"(sbase), "s"(lds)
               : "memory");
}
typedef __amdgpu_buffer_rsrc_t rsrc_t;
__device__ __forceinline__ rsrc_t make_rsrc(const void* base, u32 bytes) {
  return __builtin_amdgcn_make_buffer_rsrc((void*)base, 0, bytes, 0x00020000);
}
__device__ __forceinline__ void store16_wt(rsrc_t r, u32 off, u32x4 v) {
  __builtin_amdgcn_raw_buffer_store_b128(v, r, off, 0, 16);
}
__device__ __forceinline__ void store4_wt(rsrc_t r, u32 off, u32 v) {
  __builtin_amdgcn_raw_buffer_store_b32(v, r, off, 0, 16);
}
__device__ __forceinline__ int fresh_lane() {
  u32 z;
  asm volatile("s_mov_b32 %0, 0" : "=s"(z));
  return (int)__builtin_amdgcn_mbcnt_hi(~0u, __builtin_amdgcn_mbcnt_lo(~0u, z));
}
__device__ __forceinline__ void mem_fence() { asm volatile("" ::: "memory"); }
__device__ __forceinline__ void wait_vm0() { asm volatile("s_waitcnt vmcnt(0)" ::: "memory"); }
__device__ __forceinline__ void wait_lgkm0() { asm volatile("s_waitcnt lgkmcnt(0)" ::: "memory"); }
__device__ __forceinline__ void wg_barrier() {
  asm volatile("s_waitcnt lgkmcnt(0)\n\ts_barrier" ::: "memory");
}
__device__ __forceinline__ bf16x8 lds_read16(u32 addr) { return *(const LDSP bf16x8*)(size_t)addr; }
__device__ __forceinline__ u32x4 lds_read16u(u32 addr) { return *(const LDSP u32x4*)(size_t)addr; }
__device__ __forceinline__ s16x4 lds_read_tr(u32 addr) {
  return __builtin_amdgcn_ds_read_tr16_b64_v4i16((LDSP s16x4*)(size_t)addr);
}
__device__ __forceinline__ bf16x8 join8(s16x4 lo, s16x4 hi) {
  u32x2 a = __builtin_bit_cast(u32x2, lo), b = __builtin_bit_cast(u32x2, hi);
  u32x4 r = {a.x, a.y, b.x, b.y};
  return __builtin_bit_cast(bf16x8, r);
}

#define STAMP(wsp, kid, i)
#define CSTAMP(wsp, kid, i, extra)
#define LSTAMP(wsp, kid, i)

constexpr int K1_LDS = 163840;
constexpr int NGEMM = 192;

__device__ __forceinline__ u32 wave_scan(u32 v) {
  v += __builtin_amdgcn_update_dpp(0u, v, 0x111, 0xF, 0xF, false);
  v += __builtin_amdgcn_update_dpp(0u, v, 0x112, 0xF, 0xF, false);
  v += __builtin_amdgcn_update_dpp(0u, v, 0x114, 0xF, 0xF, false);
  v += __builtin_amdgcn_update_dpp(0u, v, 0x118, 0xF, 0xF, false);
  v += __builtin_amdgcn_update_dpp(0u, v, 0x142, 0xA, 0xF, false);
  v += __builtin_amdgcn_update_dpp(0u, v, 0x143, 0xC, 0xF, false);
  return v;
}

__device__ __forceinline__ void producer_block(char* smem, int pb, const int* __restrict__ src,
                                               const int* __restrict__ dst, const char* __restrict__ eattr,
                                               const char* __restrict__ gw, char* __restrict__ ws) {
  u32* hist = (u32*)smem;
  u32* wsum = hist + 512;
  u32* srec = wsum + 16;
  u16* seid = (u16*)(srec + 2560);
  u16* sgrp = seid + 2560;
  constexpr u32 EL = 32768;
  const int tid = threadIdx.x, lane = tid & 63, w = tid >> 6;
  const int e0 = pb * CHUNK;
  const rsrc_t wsr = make_rsrc(ws, (u32)WS_END);
  STAMP(ws, 0, 0);
  int dd[5], ss[5];
#pragma unroll
  for (int k = 0; k < 5; ++k) {
    const int i = tid + 512 * k;
    const int ii = i < CHUNK ? i : 0;
    dd[k] = dst[e0 + ii];
    ss[k] = src[e0 + ii];
  }
  hist[tid] = 0;
  wg_barrier();
  STAMP(ws, 0, 1);
  u32 rec[5], tl[5], rk[5];
#pragma unroll
  for (int k = 0; k < 5; ++k) {
    const int i = tid + 512 * k;
    const int t = tile_of(dd[k]);
    const u32 dl = dd[k] - tile_start(t);
    rec[k] = ((u32)ss[k] << 8) | (((dl >> 2) & 3) << 22) | (dl << 24) | ((u32)(t & 7) << 29);
    tl[k] = t;
    rk[k] = 0;
    if (i < CHUNK) rk[k] = atomicAdd(&hist[t], 1u);
  }
  {
    const char* ein = eattr + (size_t)e0 * 32;
    const u32 lds0 = (u32)(size_t)(LDSP char*)smem;
    const int wu = __builtin_amdgcn_readfirstlane(w);
#pragma unroll
    for (int k = 0; k < 10; ++k) {
      const int i = tid + 512 * k;
      dma16(ein, (u32)(i < 2 * CHUNK ? i : 0) * 16u, lds0 + EL + (wu * 64 + 512 * k) * 16);
    }
  }
  wg_barrier();
  const u32 c = hist[tid];
  const u32 inc = wave_scan(c);
  if (lane == 63) wsum[w] = inc;
  wg_barrier();
  u32 wb = 0;
#pragma unroll
  for (int j = 0; j < 8; ++j) wb += j < w ? wsum[j] : 0u;
  const u32 base = wb + inc - c;
  hist[tid] = base;
  u16* off = (u16*)(ws + WS_OFF) + pb * OFFP;
  off[tid] = (u16)base;
  if (tid < OFFP - 512) off[512 + tid] = (u16)CHUNK;
  wg_barrier();
#pragma unroll
  for (int k = 0; k < 5; ++k) {
    const int i = tid + 512 * k;
    if (i < CHUNK) {
      const u32 p = hist[tl[k]] + rk[k];
      srec[p] = rec[k];
      seid[p] = (u16)i;
      sgrp[p] = (u16)(tl[k] >> 3);
    }
  }
  wait_vm0();
  wg_barrier();
  STAMP(ws, 0, 2);
#pragma unroll
  for (int k = 0; k < 10; ++k) {
    const int i = tid + 512 * k;
    if (i < 2 * CHUNK)
      store16_wt(wsr, (u32)WS_EP + (u32)e0 * 32u + i * 16,
                 *(const u32x4*)(smem + EL + (int)seid[i >> 1] * 32 + (i & 1) * 16));
  }
#pragma unroll
  for (int k = 0; k < 5; ++k) {
    const int p = tid + 512 * k;
    if (p < CHUNK) {
      const u32 g = sgrp[p], rel = p - hist[g * 8];
      if (rel < (u32)CAPG) store4_wt(wsr, (u32)WS_SLOT + ((g * NPROD + pb) * CAPG + rel) * 4u, srec[p]);
    }
  }
  if (tid < CHUNK / 4) store16_wt(wsr, (u32)WS_REC + (u32)e0 * 4u + tid * 16, *(const u32x4*)(srec + tid * 4));
  if (tid + 512 < CHUNK / 4)
    store16_wt(wsr, (u32)WS_REC + (u32)e0 * 4u + (tid + 512) * 16, *(const u32x4*)(srec + (tid + 512) * 4));
  STAMP(ws, 0, 3);
  if (pb == 1) {
    const int sl = tid >> 7, ct = (tid >> 6) & 1, hh = lane >> 5, ff = lane & 31;
    u32x4 v;
#pragma unroll
    for (int j = 0; j < 4; ++j) {
      const u16* wrow = (const u16*)gw + (512 + 8 * hh + 2 * j) * D + sl * 64 + ct * 32 + ff;
      v[j] = pack_bf16(bf16_bits_to_f32(wrow[0]) * NLOG2E, bf16_bits_to_f32(wrow[D]) * NLOG2E);
    }
    store16_wt(wsr, (u32)WS_CWF + tid * 16, v);
  }
  if (pb == 0) {
    const u32x4 z = {0, 0, 0, 0};
    if (tid < 64) *(u32x4*)(ws + WS_QV + (tid >> 4) * SLICEB + (size_t)N * 256 + (tid & 15) * 16) = z;
    if (tid >= 64 && tid < 66) *(u32x4*)(ws + WS_EP + (size_t)E * 32 + (tid - 64) * 16) = z;
  }
}

__device__ __forceinline__ void gemm_block(char* smem, int gb, const char* __restrict__ x,
                                           const char* __restrict__ uw, const char* __restrict__ vw,
                                           const char* __restrict__ gw, char* __restrict__ ws) {
  const int tid = threadIdx.x, lane = tid & 63;
  const int w = __builtin_amdgcn_readfirstlane(tid >> 6);
  const u32 lds0 = (u32)(size_t)(LDSP char*)smem;
  const int q = gb >> 3, xcd = gb & 7;
  const int cg = q / 3;
  const int type = cg & 3, half = cg >> 2;
  const int rp0 = (q - 3 * cg) * 8 + xcd;
  const int cw4 = w & 3, rg = w >> 2;
  const char* W = (type == 0 ? gw + 256 * 512 : type == 1 ? vw : type == 2 ? gw : uw) + half * 256;
  const float scale = (type & 1) ? 1.0f : NLOG2E;
  const int ctg = half * 4 + cw4;
  const u32 dest = (u32)(type < 2 ? WS_QV : WS_PU) + (type & 1) * 128 + (u32)(ctg >> 1) * (u32)SLICEB;
  const rsrc_t wsr = make_rsrc(ws, (u32)WS_END);

  constexpr u32 TB = 131072;
  const bool loader = (w & 1) == 0;
  auto load_x1 = [&](int job, int i) {
    const u32 xb = ((job + 2) & 3) * 32768 + rg * 16384;
    const int j = (cw4 >> 1) * 8 + i;
    const int row = 2 * j + (lane >> 5);
    int node = (rp0 + 24 * job) * 64 + rg * 32 + row;
    node = node < N ? node : N - 1;
    const u32 voff = (u32)node * 512u + (u32)(((lane & 31) ^ (row & 15)) * 16);
    dma16(x, voff, lds0 + xb + j * 1024);
  };
  auto load_x = [&](int job) {
    if (loader) {
#pragma unroll
      for (int i = 0; i < 8; ++i) load_x1(job, i);
    }
  };
  STAMP(ws, 0, 0);
#pragma unroll
  for (int i = 0; i < 8; ++i) {
    const int j = w * 8 + i;
    const int k = 4 * j + (lane >> 4);
    const u32 voff = (u32)k * 512u + (u32)(((lane & 15) ^ ((k & 3) << 2)) * 16);
    dma16(W, voff, lds0 + j * 1024);
  }
  load_x(0);
  if (loader) asm volatile("s_waitcnt vmcnt(8)" ::: "memory"); else wait_vm0();
  wg_barrier();
  load_x(1);
  STAMP(ws, 0, 1);
  bf16x8 wf[16];
  const int h = lane >> 5, blk = (lane >> 4) & 1, qq = (lane & 15) >> 2, p = lane & 3;
  {
    const int col0 = cw4 * 32 + 16 * (p & 1) + 8 * blk + 4 * (p >> 1);
    const u32 a0 = lds0 + (8 * h + qq) * 256 + ((((col0 >> 3) ^ (qq << 2)) << 4) | ((col0 & 7) << 1));
#pragma unroll
    for (int ks = 0; ks < 16; ++ks)
      wf[ks] = join8(lds_read_tr(a0 + ks * 4096), lds_read_tr(a0 + ks * 4096 + 1024));
  }
  asm volatile("s_waitcnt vmcnt(8)" ::: "memory");
  wg_barrier();
  load_x(2);
  STAMP(ws, 0, 2);

  const int n = lane & 31;
  u32 xaddr[8];
#pragma unroll
  for (int c = 0; c < 8; ++c) xaddr[c] = lds0 + rg * 16384 + n * 512 + (((2 * c + h) ^ (n & 15)) << 4);
  auto xfrag = [&](int job, int ks) { return lds_read16(xaddr[ks & 7] + ((job + 2) & 3) * 32768 + (ks >> 3) * 256); };
  const u32 tb = lds0 + TB + (w >> 1) * 8192;
  const int cpar = w & 1;
  const u32 tput = tb + n * 128, tsw = (4 * cpar + 2 * h) ^ (n & 7);
  const u32 tget = tb + (lane >> 3) * 128 + (((lane & 7) ^ (lane >> 3)) << 4);
  f32x16 accs[2];
  bf16x8 xf[8];
#pragma unroll
  for (int ks = 0; ks < 4; ++ks) xf[ks] = xfrag(0, ks);

  auto job_body = [&](auto jc) {
    constexpr int job = decltype(jc)::value;
    f32x16& acc = accs[job & 1];
    const f32x16& accp = accs[(job + 1) & 1];
    u32x4 c0, c1, o[4];
    if (loader) {
      if (job < 5) asm volatile("s_waitcnt vmcnt(8)" ::: "memory");
      if (job == 5) wait_vm0();
    }
    wg_barrier();
    auto gap = [&](auto kc) {
      constexpr int ks = decltype(kc)::value;
      if (job < 7) {
        if (ks == 0) {
          const f32x16 zero = {0};
          acc = mfma(wf[0], xf[0], zero);
        } else {
          acc = mfma(wf[ks], xf[ks & 7], acc);
        }
        asm volatile("" : "+v"(acc));
        __builtin_amdgcn_sched_barrier(0);
        if (ks < 12) xf[(ks + 4) & 7] = xfrag(job, ks + 4);
        else if (job < 6) xf[(ks + 4) & 7] = xfrag(job + 1, ks - 12);
      }
      if (job >= 1 && job <= 7) {
        if (ks >= 1 && ks <= 4) {
          constexpr int i = ks - 1;
          c0[i] = pack_bf16(accp[2 * i] * scale, accp[2 * i + 1] * scale);
          c1[i] = pack_bf16(accp[8 + 2 * i] * scale, accp[8 + 2 * i + 1] * scale);
        }
        if (ks == 5) {
          const u32 t = tput + ((job - 1) & 1) * 4096;
          *(LDSP u32x4*)(size_t)(t + (tsw << 4)) = c0;
          *(LDSP u32x4*)(size_t)(t + ((tsw ^ 1) << 4)) = c1;
        }
      }
      if (job >= 2 && !loader) {
        if (ks == 0) {
#pragma unroll
          for (int i = 0; i < 4; ++i) o[i] = lds_read16u(tget + ((job - 2) & 1) * 4096 + i * 1024);
        }
        if (ks >= 6 && ks <= 9) {
          constexpr int i = (ks - 6) & 3;
          const int rp = rp0 + 24 * (job - 2);
          if (job - 2 < 6 || rp * 64 < N) {
            const int nn = 8 * i + (lane >> 3);
            int node = rp * 64 + rg * 32 + nn;
            node = node < N ? node : N + 1 + (nn & 7);
            store16_wt(wsr, dest + (u32)node * 256u + (lane & 7) * 16, o[i]);
          }
        }
      }
      if (job <= 3 && ks >= 6 && ks <= 13 && loader) load_x1(job + 3, ks - 6);
      __builtin_amdgcn_sched_barrier(0);
    };
    gap(IC<0>{}); gap(IC<1>{}); gap(IC<2>{}); gap(IC<3>{}); gap(IC<4>{}); gap(IC<5>{}); gap(IC<6>{}); gap(IC<7>{});
    gap(IC<8>{}); gap(IC<9>{}); gap(IC<10>{}); gap(IC<11>{}); gap(IC<12>{}); gap(IC<13>{}); gap(IC<14>{});
    gap(IC<15>{});
    STAMP(ws, 0, 3 + (job < 7 ? job : 6));
  };
  job_body(IC<0>{}); job_body(IC<1>{}); job_body(IC<2>{}); job_body(IC<3>{}); job_body(IC<4>{});
  job_body(IC<5>{}); job_body(IC<6>{}); job_body(IC<7>{}); job_body(IC<8>{});
}

__global__ void __launch_bounds__(512, 2)
prep_kernel(const char* __restrict__ x, const char* __restrict__ eattr, const int* __restrict__ src,
            const int* __restrict__ dst, const char* __restrict__ uw, const char* __restrict__ vw,
            const char* __restrict__ gw, char* __restrict__ ws) {
  extern __shared__ __attribute__((aligned(16))) char smem[];
  const int b = blockIdx.x;
  LSTAMP(ws, 0, 0);
  if (b < NPROD)
    producer_block(smem, b, src, dst, eattr, gw, ws);
  else
    gemm_block(smem, b - NPROD, x, uw, vw, gw, ws);
  LSTAMP(ws, 0, 1);
}

constexpr u32 L_QV = 0;
constexpr u32 L_E = 65536;
constexpr u32 L_S = L_E + 8 * 4096;
constexpr u32 L_A1 = L_S + 8 * 2048 + 128;
constexpr int LDUMMY = TPG * CAPT;
constexpr u32 L_A2 = L_A1 + (LDUMMY + 16) * 4;
constexpr u32 L_DL = L_A2 + (LDUMMY + 16) * 8;
constexpr u32 L_GS = L_DL + 8 * 64 * 4;
constexpr u32 L_GE = L_GS + 256;
constexpr u32 L_TT = L_GE + 256;
constexpr u32 K2_LDS = L_TT + 64;
static_assert(K2_LDS <= 163840, "LDS budget");

#define SB() __builtin_amdgcn_sched_barrier(0)
__device__ __forceinline__ void mfma_acc(f32x16& c, bf16x8 a, bf16x8 b) {
  c = mfma(a, b, c);
  asm volatile("" : "+v"(c));
  SB();
}
__device__ __forceinline__ void mfma_new(f32x16& c, bf16x8 a, bf16x8 b) {
  const f32x16 zero = {0};
  c = mfma(a, b, zero);
  asm volatile("" : "+v"(c));
  SB();
}
__device__ __forceinline__ void arrived4(const bf16x8 (&v)[4]) {
  asm volatile("" ::"v"(v[0]), "v"(v[1]), "v"(v[2]), "v"(v[3]) : "memory");
}
__device__ __forceinline__ void dma16x4(const void* sbase, const u32 (&vo)[4], u32 lds) {
  asm volatile(
      "s_mov_b32 m0, %5\n\ts_nop 0\n\t"
      "global_load_lds_dwordx4 %0, %4\n\t"
      "global_load_lds_dwordx4 %1, %4 offset:1024\n\t"
      "global_load_lds_dwordx4 %2, %4 offset:2048\n\t"
      "global_load_lds_dwordx4 %3, %4 offset:3072"
      :
      : "v"(vo[0]), "v"(vo[1]), "v"(vo[2]), "v"(vo[3]), "s"(sbase), "s"(lds)
      : "memory");
}
template <int A, int B>
__device__ __forceinline__ void exp_range(f32x16& d, const f32x16& s) {
#pragma unroll
  for (int i = A; i < B; ++i) d[i] = __builtin_amdgcn_exp2f(s[i]);
}
template <int A, int B>
__device__ __forceinline__ void rcp_range(f32x16& d) {
#pragma unroll
  for (int i = A; i < B; ++i) d[i] = __builtin_amdgcn_rcpf(d[i]);
}
__device__ __forceinline__ void mul16(f32x16& g, const f32x16& v) {
#pragma unroll
  for (int i = 0; i < 8; ++i) {
    const f32x2 a = (f32x2){g[2 * i], g[2 * i + 1]} * (f32x2){v[2 * i], v[2 * i + 1]};
    g[2 * i] = a.x;
    g[2 * i + 1] = a.y;
  }
  asm volatile("" : "+v"(g));
}
__device__ __forceinline__ void cvt16(u32x4& m0, u32x4& m1, const f32x16& d) {
#pragma unroll
  for (int i = 0; i < 4; ++i) {
    m0[i] = pack_bf16(d[2 * i], d[2 * i + 1]);
    m1[i] = pack_bf16(d[8 + 2 * i], d[8 + 2 * i + 1]);
  }
  asm volatile("" : "+v"(m0), "+v"(m1));
}
template <int NV>
__device__ __forceinline__ void wait_vm() { asm volatile("s_waitcnt vmcnt(%0)" ::"n"(NV) : "memory"); }
template <int NV>
__device__ __forceinline__ void wait_lgkm() { asm volatile("s_waitcnt lgkmcnt(%0)" ::"n"(NV) : "memory"); }

__global__ void __launch_bounds__(512, 2)
edge_kernel(const char* __restrict__ ws, u16* __restrict__ out) {
  extern __shared__ __attribute__((aligned(16))) char smem[];
  const int tid = threadIdx.x, lane = tid & 63;
  const int w = __builtin_amdgcn_readfirstlane(tid >> 6);
  const u32 lds0 = (u32)(size_t)(LDSP char*)smem;
  const int b = blockIdx.x, xcd = b & 7;
  const int slice = xcd & 3, group = (b >> 3) * 2 + (xcd >> 2);
  const int h = lane >> 5, f = lane & 31;
  STAMP(ws, 1, 0);
  LSTAMP(ws, 1, 0);

  const int pbk = tid >> 3, pj = tid & 7;
  u32 rec0[8], o0, o1;
  u32x4 cwr[2];
  {
    const char* sbase = ws + WS_SLOT + (size_t)group * NPROD * CAPG * 4;
    const u32 so = (u32)(pbk * CAPG + pj) * 4u;
    asm volatile(
        "global_load_dword %0, %8, %9\n\tglobal_load_dword %1, %8, %9 offset:32\n\t"
        "global_load_dword %2, %8, %9 offset:64\n\tglobal_load_dword %3, %8, %9 offset:96\n\t"
        "global_load_dword %4, %8, %9 offset:128\n\tglobal_load_dword %5, %8, %9 offset:160\n\t"
        "global_load_dword %6, %8, %9 offset:192\n\tglobal_load_dword %7, %8, %9 offset:224"
        : "=&v"(rec0[0]), "=&v"(rec0[1]), "=&v"(rec0[2]), "=&v"(rec0[3]), "=&v"(rec0[4]), "=&v"(rec0[5]),
          "=&v"(rec0[6]), "=&v"(rec0[7])
        : "v"(so), "s"(sbase)
        : "memory");
    const char* obase = ws + WS_OFF + (group * TPG + w) * 2;
    const u32 oo = (u32)lane * (OFFP * 2);
    asm volatile("global_load_ushort %0, %2, %3\n\tglobal_load_ushort %1, %2, %3 offset:2"
                 : "=&v"(o0), "=&v"(o1)
                 : "v"(oo), "s"(obase)
                 : "memory");
    const char* cbase = ws + WS_CWF + slice * 2048;
    const u32 co = (u32)lane * 16u;
    asm volatile("global_load_dwordx4 %0, %2, %3\n\tglobal_load_dwordx4 %1, %2, %3 offset:1024"
                 : "=&v"(cwr[0]), "=&v"(cwr[1])
                 : "v"(co), "s"(cbase)
                 : "memory");
  }
  {
    const int tile = group * TPG + w;
    const int n0w = tile_start(tile), tszw = tile_start(tile + 1) - n0w;
    const char* pu = ws + WS_PU + (size_t)slice * SLICEB;
    const u32 rb = lds0 + L_QV + w * 8192;
#pragma unroll
    for (int j = 0; j < 4; ++j) {
      const int row = 8 * j + (lane >> 3);
      const int node = n0w + (row < tszw ? row : tszw - 1);
      const u32 voff = (u32)node * 256u + (lane & 7) * 16;
      dma16(pu, voff, rb + j * 1024);
    }
  }
  {
    const u32x4 z = {0, 0, 0, 0};
    *(LDSP u32x4*)(size_t)(lds0 + L_S + w * 2048 + lane * 16) = z;
    *(LDSP u32x4*)(size_t)(lds0 + L_S + w * 2048 + 1024 + lane * 16) = z;
  }
  bf16x8 cw[2], sel[2], one;
#pragma unroll
  for (int s = 0; s < 2; ++s)
#pragma unroll
    for (int j = 0; j < 8; ++j) sel[s][j] = (__bf16)(f == 16 * s + 8 * h + j ? 1.0f : 0.0f);
#pragma unroll
  for (int j = 0; j < 8; ++j) one[j] = (__bf16)((j == 0 && h == 0) ? 1.0f : 0.0f);
  asm volatile("" : "+v"(sel[0]), "+v"(sel[1]), "+v"(one));
  SB();
  asm volatile("s_waitcnt vmcnt(4)"
               : "+v"(rec0[0]), "+v"(rec0[1]), "+v"(rec0[2]), "+v"(rec0[3]), "+v"(rec0[4]), "+v"(rec0[5]),
                 "+v"(rec0[6]), "+v"(rec0[7]), "+v"(o0), "+v"(o1), "+v"(cwr[0]), "+v"(cwr[1])
               :
               : "memory");
  SB();
  cw[0] = __builtin_bit_cast(bf16x8, cwr[0]);
  cw[1] = __builtin_bit_cast(bf16x8, cwr[1]);
  const int cnt = (int)o1 - (int)o0;
  const int inc = (int)wave_scan((u32)cnt);
  int* dl = (int*)(smem + L_DL);
  dl[w * 64 + lane] = inc - cnt - (int)o0;
  if (w == 0) ((int*)(smem + L_GS))[lane] = (int)o0;
  if (w == 7) ((int*)(smem + L_GE))[lane] = (int)o1;
  if (lane == 63) ((int*)(smem + L_TT))[w] = inc;
  STAMP(ws, 1, 1);
  wg_barrier();
  STAMP(ws, 1, 2);
  LSTAMP(ws, 1, 6);

  const int* tt = (const int*)(smem + L_TT);
  int tau, mytot, maxt;
  {
    const int ti = tt[lane >> 3], tj = tt[lane & 7];
    const bool before = ti > tj || (ti == tj && (lane >> 3) < (lane & 7));
    const unsigned long long m = __ballot(before);
    const int rank = __popcll(m & (0x0101010101010101ull << (lane & 7)));
    const int want = w < 4 ? w : 11 - w;
    tau = __builtin_ctzll(__ballot(lane < 8 && rank == want));
    mytot = __builtin_amdgcn_readlane(tj, tau);
    maxt = __builtin_amdgcn_readlane(tj, __builtin_ctzll(__ballot(lane < 8 && rank == 0)));
  }
  const int npass = maxt > CAPT ? (maxt + CAPT - 1) / CAPT : 1;
  const int tile = group * TPG + tau;
  const int n0 = tile_start(tile), tsz = tile_start(tile + 1) - n0;
  const u32 qb = lds0 + L_QV + tau * 8192, vb = qb + 4096;
  const u32* grec = (const u32*)(ws + WS_REC);

  u32 cq[4];
#pragma unroll
  for (int j = 0; j < 4; ++j) cq[j] = (u32)(((lane & 7) ^ (4 * (j & 1) + (lane >> 4))) * 16 + 3072 - 1024 * j);
  const u32 ce = (u32)(((lane & 1) ^ ((lane >> 4) & 1)) * 16);
  const char* qsrc = ws + WS_QV + (size_t)slice * SLICEB - 3072;
  const char* vsrc = qsrc + 128;
  const char* esrc = ws + WS_EP;
  const u32 eb = lds0 + L_E + tau * 4096;
  const u32 a1 = lds0 + L_A1 + tau * CAPT * 4 + (lane >> 3) * 16;
  const u32 a2 = lds0 + L_A2 + tau * CAPT * 8 + (lane >> 1) * 8;
  const u32 sb = lds0 + L_S + tau * 2048;
  const u32 simg = lds0 + L_S;
  const int sr = (f & 0x13) | ((f & 4) << 1) | ((f & 8) >> 1);
  const int sq = ((sr & 7) << 2) | (sr >> 3);
  const int fsw = (sr >> 1) & 7;
  const u32 eaddr = eb + sq * 32 + ((h ^ ((sq >> 3) & 1)) << 4);
  u32 qaddr[4];
#pragma unroll
  for (int c = 0; c < 4; ++c) qaddr[c] = qb + sr * 128 + (((2 * c + h) ^ fsw) << 4);
  u32 saddr[2], staddr[2];
  {
    const int blk = (lane >> 4) & 1, qq = (lane & 15) >> 2, p = lane & 3;
    const int c0 = 16 * blk + 8 * (p & 1) + 4 * (p >> 1);
#pragma unroll
    for (int s = 0; s < 2; ++s) {
      saddr[s] = sb + f * 64 + (((2 * s + h) ^ ((f >> 2) & 3)) << 4);
      const int nu = 8 * s + 4 * h + qq;
      staddr[s] = sb + nu * 64 + (((c0 >> 3) ^ ((nu >> 2) & 3)) << 4) + ((c0 & 7) << 1);
    }
  }
  auto read_st = [&](int s) { return join8(lds_read_tr(staddr[0] + s * 1024), lds_read_tr(staddr[1] + s * 1024)); };
  STAMP(ws, 1, 3);
  LSTAMP(ws, 1, 7);

  auto build_lists = [&](int pass, bool first) {
    u32* A1 = (u32*)(smem + L_A1);
    u32x2* A2 = (u32x2*)(smem + L_A2);
    const int tb = threadIdx.x >> 3, tj = threadIdx.x & 7;
    const int bs0 = ((const int*)(smem + L_GS))[tb], be0 = ((const int*)(smem + L_GE))[tb];
    const int* dlb = dl + tb;
    const int pos0 = tb * CHUNK;
    auto put = [&](u32 rec, int i, int d) {
      const u32 li = (u32)(i + d - pass * CAPT);
      const bool ok = i < be0 && li < (u32)CAPT;
      const u32 idx = ok ? (rec >> 29) * CAPT + li : (u32)(LDUMMY + (threadIdx.x & 15));
      A1[idx] = rec & 0x3FFF00u;
      const u32 sa = ((rec >> 18) & 0x3FF0u) ^ (((li & 3) << 4) | ((li >> 1) & 0xE));
      A2[idx] = (u32x2){(u32)(pos0 + i) << 5, sa};
    };
    int i0 = bs0 + tj;
    if (first) {
      int d[8];
#pragma unroll
      for (int k = 0; k < 8; ++k) d[k] = dlb[(rec0[k] >> 29) * 64];
#pragma unroll
      for (int k = 0; k < 8; ++k) put(rec0[k], bs0 + tj + 8 * k, d[k]);
      i0 += CAPG;
    }
    for (int i = i0; i < be0; i += 8) {
      const u32 rec = grec[pos0 + i];
      put(rec, i, dlb[(rec >> 29) * 64]);
    }
    int tw = ((const int*)(smem + L_TT))[w] - pass * CAPT;
    tw = tw < 0 ? 0 : tw;
    const int ln = threadIdx.x & 63;
    if (tw < CAPT && ln < 32) {
      const int li = (tw & ~31) + ln;
      if (li >= tw) {
        A1[w * CAPT + li] = (u32)N * 256u;
        A2[w * CAPT + li] = (u32x2){(u32)E * 32u, 8u * 2048u};
      }
    }
  };
  build_lists(0, true);
  wait_vm0();
  STAMP(ws, 1, 4);
  wg_barrier();
  bf16x8 pb[2][2];
  {
    const int blk = (lane >> 4) & 1, qq = (lane & 15) >> 2, p = lane & 3;
#pragma unroll
    for (int ct = 0; ct < 2; ++ct)
#pragma unroll
      for (int s = 0; s < 2; ++s) {
        const u32 a = qb + (16 * s + 4 * h + qq) * 128 + (ct * 32 + 16 * blk + 4 * p) * 2;
        pb[ct][s] = join8(lds_read_tr(a), lds_read_tr(a + 8 * 128));
      }
  }
  f32x16 agg[2] = {{0}, {0}};
  auto u_offsets = [&](u32 (&o)[4]) {
    const int ln = fresh_lane();
#pragma unroll
    for (int j = 0; j < 4; ++j) {
      const int row = 8 * j + (ln >> 3);
      o[j] = (u32)(WS_PU - WS_QV) + (u32)(n0 + (row < tsz ? row : tsz - 1)) * 256u + cq[j];
    }
  };

  for (int pass = 0; pass < npass; ++pass) {
    if (pass > 0) {
      build_lists(pass, false);
      wg_barrier();
    }
    STAMP(ws, 1, 5);
    LSTAMP(ws, 1, 1);

    int rem = mytot - pass * CAPT;
    rem = rem < 0 ? 0 : (rem > CAPT ? CAPT : rem);
    const int nsteps = __builtin_amdgcn_readfirstlane(rem > 0 ? (rem + 31) >> 5 : 1);
    {
      const int last = nsteps - 1;
      auto clampt = [&](int t) { return t < last ? t : last; };
      auto rd16 = [&](int t) -> u32 { return *(const LDSP u32*)(size_t)(a2 + t * 256 + 4); };
      auto rd32 = [&](int t) -> u32 { return *(const LDSP u32*)(size_t)(a2 + t * 256); };
      auto s_write = [&](u32 a, u16 v) { *(LDSP u16*)(size_t)(simg + a) = v; };
      auto dma_e = [&](u32 eo, int t) { dma16(esrc, eo + ce, eb + (t & 3) * 1024); };
      u32 vo[4];
      auto set_vo = [&](const u32x4& so) {
#pragma unroll
        for (int j = 0; j < 4; ++j) vo[j] = so[j] + cq[j];
      };
      const f32x16 zero = {0};

      set_vo(lds_read16u(a1));
      dma_e(rd32(0), 0);
      dma16x4(qsrc, vo, qb);
      dma16x4(vsrc, vo, vb);
      dma_e(rd32(clampt(1)), 1);
      dma_e(rd32(clampt(2)), 2);
      u32 sa = rd16(0);
      u32 sa1 = rd16(clampt(1));
      s_write(sa, (u16)0x3F80);
      mem_fence();
      bf16x8 stf[2], sf[2], qf[4], vfr[4], ef;
#pragma unroll
      for (int s = 0; s < 2; ++s) stf[s] = read_st(s);
      const u32x4 so1 = lds_read16u(a1 + clampt(1) * 128);
      const u32 eo3 = rd32(clampt(3));
      wait_vm<6>();
      ef = lds_read16(eaddr);
#pragma unroll
      for (int c = 0; c < 4; ++c) qf[c] = lds_read16(qaddr[c]);
      set_vo(so1);
      arrived4(qf);
      dma16x4(qsrc, vo, qb);
      f32x16 za, zb, ea, ebv = zero, vf;
      za = mfma(ef, cw[0], zero);
      zb = mfma(ef, cw[1], zero);
      za = mfma(stf[0], pb[0][0], za);
      zb = mfma(stf[0], pb[1][0], zb);
      za = mfma(stf[1], pb[0][1], za);
      zb = mfma(stf[1], pb[1][1], zb);
      za = mfma(qf[0], sel[0], za);
      zb = mfma(qf[2], sel[0], zb);
      za = mfma(qf[1], sel[1], za);
      u32x4 m0 = {0, 0, 0, 0}, m1 = {0, 0, 0, 0};
      sf[0] = __builtin_bit_cast(bf16x8, m0);
      sf[1] = sf[0];
      dma_e(eo3, 3);
      SB();

      LSTAMP(ws, 1, 4);
      for (int t = 0; t <= last; ++t) {
        wait_vm<5>();
#pragma unroll
        for (int c = 0; c < 4; ++c) vfr[c] = lds_read16(qaddr[c] + 4096);
        SB();
        const u32x4 so = lds_read16u(a1 + clampt(t + 2) * 128);
        const u32 eo = rd32(clampt(t + 4));
        const u32 sa2 = rd16(clampt(t + 2));
        SB();
        mfma_acc(zb, qf[3], sel[1]);
        cvt16(m0, m1, ebv);
        SB();
        mfma_acc(agg[1], sf[0], __builtin_bit_cast(bf16x8, m0));
        exp_range<0, 4>(ea, za);
        SB();
        mfma_acc(agg[1], sf[1], __builtin_bit_cast(bf16x8, m1));
        sf[0] = lds_read16(saddr[0]);
        sf[1] = lds_read16(saddr[1]);
        mem_fence();
        s_write(sa, (u16)0);
        s_write(sa1, (u16)0x3F80);
        mem_fence();
        stf[0] = read_st(0);
        stf[1] = read_st(1);
        exp_range<4, 8>(ea, za);
        SB();
        mfma_new(vf, vfr[0], sel[0]);
        ef = lds_read16(eaddr + ((t + 1) & 3) * 1024);
        exp_range<8, 12>(ea, za);
        SB();
        mfma_acc(vf, vfr[1], sel[1]);
        arrived4(vfr);
        if (t == last && pass + 1 == npass) u_offsets(vo);
        dma16x4(vsrc, vo, vb);
        wait_vm<5>();
#pragma unroll
        for (int c = 0; c < 4; ++c) qf[c] = lds_read16(qaddr[c]);
        sa = sa1;
        sa1 = sa2;
        exp_range<12, 16>(ea, za);
        SB();
        __builtin_amdgcn_s_setprio(0);
        set_vo(so);
        exp_range<0, 2>(ebv, zb);
        SB();
        mfma_acc(ea, one, one);
        exp_range<2, 5>(ebv, zb);
        SB();
        mfma_new(za, ef, cw[0]);
        exp_range<5, 8>(ebv, zb);
        SB();
        mfma_acc(za, stf[0], pb[0][0]);
        arrived4(qf);
        dma16x4(qsrc, vo, qb);
        dma_e(eo, t + 4);
        exp_range<8, 11>(ebv, zb);
        SB();
        mfma_acc(za, stf[1], pb[0][1]);
        exp_range<11, 14>(ebv, zb);
        SB();
        mfma_acc(za, qf[0], sel[0]);
        exp_range<14, 16>(ebv, zb);
        SB();
        rcp_range<0, 1>(ea);
        SB();
        mfma_acc(ebv, one, one);
        rcp_range<1, 4>(ea);
        SB();
        mfma_acc(za, qf[1], sel[1]);
        rcp_range<4, 7>(ea);
        SB();
        mfma_new(zb, ef, cw[1]);
        rcp_range<7, 10>(ea);
        SB();
        mfma_acc(zb, stf[0], pb[1][0]);
        rcp_range<10, 13>(ea);
        SB();
        mfma_acc(zb, stf[1], pb[1][1]);
        rcp_range<13, 16>(ea);
        SB();
        mul16(ea, vf);
        SB();
        __builtin_amdgcn_s_setprio(1);
        mfma_new(vf, vfr[2], sel[0]);
        cvt16(m0, m1, ea);
        SB();
        mfma_acc(vf, vfr[3], sel[1]);
        rcp_range<0, 4>(ebv);
        SB();
        mfma_acc(agg[0], sf[0], __builtin_bit_cast(bf16x8, m0));
        rcp_range<4, 8>(ebv);
        SB();
        mfma_acc(agg[0], sf[1], __builtin_bit_cast(bf16x8, m1));
        rcp_range<8, 12>(ebv);
        SB();
        mfma_acc(zb, qf[2], sel[0]);
        rcp_range<12, 16>(ebv);
        SB();
        mul16(ebv, vf);
        SB();
      }
      __builtin_amdgcn_s_setprio(0);
      cvt16(m0, m1, ebv);
      LSTAMP(ws, 1, 5);
      agg[1] = mfma(sf[0], __builtin_bit_cast(bf16x8, m0), agg[1]);
      agg[1] = mfma(sf[1], __builtin_bit_cast(bf16x8, m1), agg[1]);
      s_write(sa, (u16)0);
      wait_vm0();
    }
    if (pass + 1 < npass) wg_barrier();
  }
  STAMP(ws, 1, 6);
  LSTAMP(ws, 1, 2);

  {
    const int lane = fresh_lane(), h = lane >> 5, f = lane & 31;
    bf16x8 ident[2];
#pragma unroll
    for (int s = 0; s < 2; ++s)
#pragma unroll
      for (int j = 0; j < 8; ++j)
        ident[s][j] = (__bf16)(f == 16 * s + 8 * (j >> 2) + 4 * h + (j & 3) ? 1.0f : 0.0f);
    const int blk = (lane >> 4) & 1, qq = (lane & 15) >> 2, p = lane & 3;
#pragma unroll
    for (int ct = 0; ct < 2; ++ct)
#pragma unroll
      for (int s = 0; s < 2; ++s) {
        s16x4 part[2];
#pragma unroll
        for (int t = 0; t < 2; ++t) {
          const int row = 16 * s + 8 * t + 4 * h + qq;
          const int ch = 4 * ct + 2 * blk + (p >> 1);
          part[t] = lds_read_tr(vb + row * 128 + ((ch ^ ((row >> 1) & 7)) << 4) + (p & 1) * 8);
        }
        agg[ct] = mfma(ident[s], join8(part[0], part[1]), agg[ct]);
      }
  }
  {
  const int lane = fresh_lane(), h = lane >> 5, f = lane & 31;
#pragma unroll
  for (int ct = 0; ct < 2; ++ct)
#pragma unroll
    for (int r = 0; r < 12; r += 2) {
      const float a0 = agg[ct][r], a1v = agg[ct][r + 1];
      const float g0 = __builtin_amdgcn_rcpf(1.0f + __builtin_amdgcn_exp2f(a0 * NLOG2E));
      const float g1 = __builtin_amdgcn_rcpf(1.0f + __builtin_amdgcn_exp2f(a1v * NLOG2E));
      const u32 pk = pack_bf16(a0 * g0, a1v * g1);
      const u32 a = qb + ((r & 3) + 8 * (r >> 2) + 4 * h) * 128 + (ct * 32 + f) * 2;
      *(LDSP u16*)(size_t)a = (u16)pk;
      *(LDSP u16*)(size_t)(a + 128) = (u16)(pk >> 16);
    }
  mem_fence();
  {
    const rsrc_t outr = make_rsrc(out, (u32)N * D * 2);
#pragma unroll
    for (int i = 0; i < 3; ++i) {
      const int row = 8 * i + (lane >> 3);
      const u32x4 v = lds_read16u(qb + row * 128 + (lane & 7) * 16);
      if (row < tsz) store16_wt(outr, (u32)(n0 + row) * (D * 2) + slice * 128 + (lane & 7) * 16, v);
    }
  }
  }
  STAMP(ws, 1, 7);
  LSTAMP(ws, 1, 3);
  STAMP(ws, 1, 8);
}

inline void launch_k1(void* const* d_in, void* d_ws, hipStream_t stream) {
  (void)hipFuncSetAttribute((const void*)prep_kernel, hipFuncAttributeMaxDynamicSharedMemorySize, K1_LDS);
  prep_kernel<<<NPROD + NGEMM, 512, K1_LDS, stream>>>((const char*)d_in[0], (const char*)d_in[1], (const int*)d_in[2],
                                                    (const int*)d_in[3], (const char*)d_in[4], (const char*)d_in[5],
                                                    (const char*)d_in[6], (char*)d_ws);
}
inline void launch_k2(void* const* d_in, void* d_out, void* d_ws, hipStream_t stream) {
  constexpr u32 K2_DYN = K2_LDS;
  (void)hipFuncSetAttribute((const void*)edge_kernel, hipFuncAttributeMaxDynamicSharedMemorySize, K2_DYN);
  edge_kernel<<<256, 512, K2_DYN, stream>>>((const char*)d_ws, (u16*)d_out);
}

}

extern "C" void kernel_launch(void* const* d_in, const int* in_sizes, int n_in, void* d_out, int out_size,
                              void* d_ws, size_t ws_size, hipStream_t stream) {
  egc::launch_k1(d_in, d_ws, stream);
  egc::launch_k2(d_in, d_out, d_ws, stream);
}
```

```cpp
#include <hip/hip_runtime.h>
#include <hip/hip_bf16.h>
#include <stdint.h>

namespace egc {

typedef unsigned int u32;
typedef unsigned short u16;
typedef float f32x2 __attribute__((ext_vector_type(2)));
typedef float f32x16 __attribute__((ext_vector_type(16)));
typedef __bf16 bf16x2 __attribute__((ext_vector_type(2)));
typedef __bf16 bf16x4 __attribute__((ext_vector_type(4)));
typedef __bf16 bf16x8 __attribute__((ext_vector_type(8)));
typedef short s16x4 __attribute__((ext_vector_type(4)));
typedef u32 u32x2 __attribute__((ext_vector_type(2)));
typedef u32 u32x4 __attribute__((ext_vector_type(4)));
#define LDSP __attribute__((address_space(3)))

constexpr int N = 10000, D = 256, DE = 16, E = 160000;
constexpr int NPROD = 64;
constexpr int CHUNK = E / NPROD;
constexpr int NTILE = 512;
constexpr int TPG = 8;
constexpr int OFFP = 520;
constexpr int NROWP = 10016;
constexpr size_t SLICEB = (size_t)NROWP * 256;
constexpr int CAPT = 448;
constexpr float NLOG2E = -1.44269504088896340736f;

constexpr size_t WS_QV = 0;
constexpr size_t WS_PU = WS_QV + 4 * SLICEB;
constexpr size_t WS_EP = WS_PU + 4 * SLICEB;
constexpr size_t WS_REC = WS_EP + (size_t)(E + 64) * 32;
constexpr size_t WS_OFF = WS_REC + (size_t)(E + 64) * 4;
constexpr int NGROUP = NTILE / TPG;
constexpr int CAPG = 64;
constexpr size_t WS_SLOT = (WS_OFF + (size_t)NPROD * OFFP * 2 + 255) & ~(size_t)255;
constexpr size_t WS_CWF = WS_SLOT + (size_t)NGROUP * NPROD * CAPG * 4;
constexpr size_t WS_END = WS_CWF + 4 * 2 * 64 * 16;

template <int V>
struct IC {
  static constexpr int value = V;
};

__device__ __forceinline__ int tile_start(int t) { return (625 * t) >> 5; }
__device__ __forceinline__ int tile_of(int d) { return (32 * d + 31) / 625; }

__device__ __forceinline__ f32x16 mfma(bf16x8 a, bf16x8 b, f32x16 c) {
  return __builtin_amdgcn_mfma_f32_32x32x16_bf16(a, b, c, 0, 0, 0);
}
__device__ __forceinline__ u32 pack_bf16(float a, float b) {
  bf16x2 v;
  v[0] = (__bf16)a;
  v[1] = (__bf16)b;
  return __builtin_bit_cast(u32, v);
}
__device__ __forceinline__ float bf16_bits_to_f32(u16 b) { return __builtin_bit_cast(float, (u32)b << 16); }

__device__ __forceinline__ void dma16(const void* sbase, u32 voff, u32 lds) {
  asm volatile("s_mov_b32 m0, %2\n\ts_nop 0\n\tglobal_load_lds_dwordx4 %0, %1"
               :
               : "v"(voff), "s"(sbase), "s"(lds)
               : "memory");
}
typedef __amdgpu_buffer_rsrc_t rsrc_t;
#define XK0 0
#define TBL_AUX 16
#define EP_AUX 16
#define OUT_AUX 16
__device__ __forceinline__ rsrc_t make_rsrc(const void* base, u32 bytes) {
  return __builtin_amdgcn_make_buffer_rsrc((void*)base, 0, bytes, 0x00020000);
}
__device__ __forceinline__ void store16_wt(rsrc_t r, u32 off, u32x4 v) {
  __builtin_amdgcn_raw_buffer_store_b128(v, r, off, 0, 16);
}
__device__ __forceinline__ void store4_wt(rsrc_t r, u32 off, u32 v) {
  __builtin_amdgcn_raw_buffer_store_b32(v, r, off, 0, 16);
}
__device__ __forceinline__ int fresh_lane() {
  u32 z;
  asm volatile("s_mov_b32 %0, 0" : "=s"(z));
  return (int)__builtin_amdgcn_mbcnt_hi(~0u, __builtin_amdgcn_mbcnt_lo(~0u, z));
}
__device__ __forceinline__ void bdma16x4(rsrc_t r, u32 v0, u32 v1, u32 v2, u32 v3, u32 soff, u32 lds) {
  asm volatile(
      "s_mov_b32 m0, %6\n\ts_nop 0\n\t"
      "buffer_load_dwordx4 %0, %4, %5 offen lds\n\t"
      "buffer_load_dwordx4 %1, %4, %5 offen offset:1024 lds\n\t"
      "buffer_load_dwordx4 %2, %4, %5 offen offset:2048 lds\n\t"
      "buffer_load_dwordx4 %3, %4, %5 offen offset:3072 lds"
      :
      : "v"(v0), "v"(v1), "v"(v2), "v"(v3), "s"(r), "s"(soff), "s"(lds)
      : "memory");
}
__device__ __forceinline__ void bdma16(rsrc_t r, u32 v, u32 soff, u32 lds) {
  asm volatile("s_mov_b32 m0, %3\n\ts_nop 0\n\tbuffer_load_dwordx4 %0, %1, %2 offen lds"
               :
               : "v"(v), "s"(r), "s"(soff), "s"(lds)
               : "memory");
}
__device__ __forceinline__ void mem_fence() { asm volatile("" ::: "memory"); }
__device__ __forceinline__ void wait_vm0() { asm volatile("s_waitcnt vmcnt(0)" ::: "memory"); }
__device__ __forceinline__ void wait_lgkm0() { asm volatile("s_waitcnt lgkmcnt(0)" ::: "memory"); }
__device__ __forceinline__ void wg_barrier() {
  asm volatile("s_waitcnt lgkmcnt(0)\n\ts_barrier" ::: "memory");
}
__device__ __forceinline__ bf16x8 lds_read16(u32 addr) { return *(const LDSP bf16x8*)(size_t)addr; }
__device__ __forceinline__ u32x4 lds_read16u(u32 addr) { return *(const LDSP u32x4*)(size_t)addr; }
__device__ __forceinline__ s16x4 lds_read_tr(u32 addr) {
  return __builtin_amdgcn_ds_read_tr16_b64_v4i16((LDSP s16x4*)(size_t)addr);
}
__device__ __forceinline__ bf16x8 join8(s16x4 lo, s16x4 hi) {
  u32x2 a = __builtin_bit_cast(u32x2, lo), b = __builtin_bit_cast(u32x2, hi);
  u32x4 r = {a.x, a.y, b.x, b.y};
  return __builtin_bit_cast(bf16x8, r);
}

#define STAMP(wsp, kid, i)
#define CSTAMP(wsp, kid, i, extra)
#define LSTAMP(wsp, kid, i)
#define LCYC(wsp, kid, i)

constexpr int K1_LDS = 163840;
constexpr int NGEMM = 192;

__device__ __forceinline__ u32 wave_scan(u32 v) {
  v += __builtin_amdgcn_update_dpp(0u, v, 0x111, 0xF, 0xF, false);
  v += __builtin_amdgcn_update_dpp(0u, v, 0x112, 0xF, 0xF, false);
  v += __builtin_amdgcn_update_dpp(0u, v, 0x114, 0xF, 0xF, false);
  v += __builtin_amdgcn_update_dpp(0u, v, 0x118, 0xF, 0xF, false);
  v += __builtin_amdgcn_update_dpp(0u, v, 0x142, 0xA, 0xF, false);
  v += __builtin_amdgcn_update_dpp(0u, v, 0x143, 0xC, 0xF, false);
  return v;
}

__device__ __forceinline__ void producer_block(char* smem, int pb, const int* __restrict__ src,
                                               const int* __restrict__ dst, const char* __restrict__ eattr,
                                               const char* __restrict__ gw, char* __restrict__ ws) {
  u32* hist = (u32*)smem;
  u32* wsum = hist + 512;
  u32* srec = wsum + 16;
  u16* seid = (u16*)(srec + 2560);
  u16* sgrp = seid + 2560;
  constexpr u32 EL = 32768;
  const int tid = threadIdx.x, lane = tid & 63, w = tid >> 6;
  const int e0 = pb * CHUNK;
  const rsrc_t wsr = make_rsrc(ws, (u32)WS_END);
  STAMP(ws, 0, 0);
  int dd[5], ss[5];
#pragma unroll
  for (int k = 0; k < 5; ++k) {
    const int i = tid + 512 * k;
    const int ii = i < CHUNK ? i : 0;
    dd[k] = dst[e0 + ii];
    ss[k] = src[e0 + ii];
  }
  hist[tid] = 0;
  wg_barrier();
  LSTAMP(ws, 0, 2);
  u32 rec[5], tl[5], rk[5];
#pragma unroll
  for (int k = 0; k < 5; ++k) {
    const int i = tid + 512 * k;
    const int t = tile_of(dd[k]);
    const u32 dl = dd[k] - tile_start(t);
    rec[k] = ((u32)ss[k] << 8) | (((dl >> 2) & 3) << 22) | (dl << 24) | ((u32)(t & 7) << 29);
    tl[k] = t;
    rk[k] = 0;
    if (i < CHUNK) rk[k] = atomicAdd(&hist[t], 1u);
  }
  {
    const char* ein = eattr + (size_t)e0 * 32;
    const u32 lds0 = (u32)(size_t)(LDSP char*)smem;
    const int wu = __builtin_amdgcn_readfirstlane(w);
#pragma unroll
    for (int k = 0; k < 10; ++k) {
      const int i = tid + 512 * k;
      dma16(ein, (u32)(i < 2 * CHUNK ? i : 0) * 16u, lds0 + EL + (wu * 64 + 512 * k) * 16);
    }
  }
  wg_barrier();
  const u32 c = hist[tid];
  const u32 inc = wave_scan(c);
  if (lane == 63) wsum[w] = inc;
  wg_barrier();
  u32 wb = 0;
#pragma unroll
  for (int j = 0; j < 8; ++j) wb += j < w ? wsum[j] : 0u;
  const u32 base = wb + inc - c;
  hist[tid] = base;
  u16* off = (u16*)(ws + WS_OFF) + pb * OFFP;
  off[tid] = (u16)base;
  if (tid < OFFP - 512) off[512 + tid] = (u16)CHUNK;
  wg_barrier();
#pragma unroll
  for (int k = 0; k < 5; ++k) {
    const int i = tid + 512 * k;
    if (i < CHUNK) {
      const u32 p = hist[tl[k]] + rk[k];
      srec[p] = rec[k];
      seid[p] = (u16)i;
      sgrp[p] = (u16)(tl[k] >> 3);
    }
  }
  LSTAMP(ws, 0, 3);
  wait_vm0();
  wg_barrier();
  LSTAMP(ws, 0, 4);
#pragma unroll
  for (int k = 0; k < 10; ++k) {
    const int i = tid + 512 * k;
    if (i < 2 * CHUNK)
      __builtin_amdgcn_raw_buffer_store_b128(*(const u32x4*)(smem + EL + (int)seid[i >> 1] * 32 + (i & 1) * 16), wsr,
                                             (u32)WS_EP + (u32)e0 * 32u + i * 16, 0, EP_AUX);
  }
#pragma unroll
  for (int k = 0; k < 5; ++k) {
    const int p = tid + 512 * k;
    if (p < CHUNK) {
      const u32 g = sgrp[p], rel = p - hist[g * 8];
      if (rel < (u32)CAPG) store4_wt(wsr, (u32)WS_SLOT + ((g * NPROD + pb) * CAPG + rel) * 4u, srec[p]);
    }
  }
  if (tid < CHUNK / 4) store16_wt(wsr, (u32)WS_REC + (u32)e0 * 4u + tid * 16, *(const u32x4*)(srec + tid * 4));
  if (tid + 512 < CHUNK / 4)
    store16_wt(wsr, (u32)WS_REC + (u32)e0 * 4u + (tid + 512) * 16, *(const u32x4*)(srec + (tid + 512) * 4));
  STAMP(ws, 0, 3);
  if (pb == 1) {
    const int sl = tid >> 7, ct = (tid >> 6) & 1, hh = lane >> 5, ff = lane & 31;
    u32x4 v;
#pragma unroll
    for (int j = 0; j < 4; ++j) {
      const u16* wrow = (const u16*)gw + (512 + 8 * hh + 2 * j) * D + sl * 64 + ct * 32 + ff;
      v[j] = pack_bf16(bf16_bits_to_f32(wrow[0]) * NLOG2E, bf16_bits_to_f32(wrow[D]) * NLOG2E);
    }
    store16_wt(wsr, (u32)WS_CWF + tid * 16, v);
  }
  if (pb == 0) {
    const u32x4 z = {0, 0, 0, 0};
    if (tid < 64) *(u32x4*)(ws + WS_QV + (tid >> 4) * SLICEB + (size_t)N * 256 + (tid & 15) * 16) = z;
    if (tid >= 64 && tid < 66) *(u32x4*)(ws + WS_EP + (size_t)E * 32 + (tid - 64) * 16) = z;
  }
}

__device__ __forceinline__ void gemm_block(char* smem, int gb, const char* __restrict__ x,
                                           const char* __restrict__ uw, const char* __restrict__ vw,
                                           const char* __restrict__ gw, char* __restrict__ ws) {
  const int tid = threadIdx.x, lane = tid & 63;
  const int w = __builtin_amdgcn_readfirstlane(tid >> 6);
  const u32 lds0 = (u32)(size_t)(LDSP char*)smem;
  const int q = gb >> 3, xcd = gb & 7;
  const int cg = q / 3;
  const int type = cg & 3, half = cg >> 2;
  const int rp0 = (q - 3 * cg) * 8 + xcd;
  const int cw4 = w & 3, rg = w >> 2;
  const char* W = (type == 0 ? gw + 256 * 512 : type == 1 ? vw : type == 2 ? gw : uw) + half * 256;
  const float scale = (type & 1) ? 1.0f : NLOG2E;
  const int ctg = half * 4 + cw4;
  const rsrc_t xr = make_rsrc(x, (u32)N * 512u);
  const rsrc_t dr = make_rsrc(ws + (type < 2 ? WS_QV : WS_PU) + (type & 1) * 128 + (size_t)(ctg >> 1) * SLICEB,
                              (u32)N * 256u - 128u);

  constexpr u32 TB = 131072;
  const u32 xlane = (u32)((lane >> 5) * 512) + ((u32)(((lane & 31) ^ (lane >> 5)) << 4) ^ (u32)((cw4 & 1) << 7));
  auto x_soff = [&](int job) { return (u32)((rp0 + 24 * job) * 64 + rg * 32) * 512u + (u32)cw4 * 4096u; };
  auto x_lds = [&](int job) { return lds0 + ((job + 2) & 3) * 32768 + rg * 16384 + cw4 * 4096; };
  auto load_x = [&](int job) { bdma16x4(xr, xlane, xlane ^ 32u, xlane ^ 64u, xlane ^ 96u, x_soff(job), x_lds(job)); };
  STAMP(ws, 0, 0);
#pragma unroll
  for (int i = 0; i < 8; ++i) {
    const int j = w * 8 + i;
    const int k = 4 * j + (lane >> 4);
    const u32 voff = (u32)k * 512u + (u32)(((lane & 15) ^ ((k & 3) << 2)) * 16);
    dma16(W, voff, lds0 + j * 1024);
  }
  load_x(0);
  asm volatile("s_waitcnt vmcnt(4)" ::: "memory");
  wg_barrier();
  load_x(1);
  LSTAMP(ws, 0, 2);
  bf16x8 wf[16];
  const int h = lane >> 5, blk = (lane >> 4) & 1, qq = (lane & 15) >> 2, p = lane & 3;
  {
    const int col0 = cw4 * 32 + 16 * (p & 1) + 8 * blk + 4 * (p >> 1);
    const u32 a0 = lds0 + (8 * h + qq) * 256 + ((((col0 >> 3) ^ (qq << 2)) << 4) | ((col0 & 7) << 1));
#pragma unroll
    for (int ks = 0; ks < 16; ++ks)
      wf[ks] = join8(lds_read_tr(a0 + ks * 4096), lds_read_tr(a0 + ks * 4096 + 1024));
  }
  asm volatile("s_waitcnt vmcnt(4)" ::: "memory");
  wg_barrier();
  load_x(2);
  LSTAMP(ws, 0, 3);

  const int n = lane & 31;
  u32 xaddr[8];
#pragma unroll
  for (int c = 0; c < 8; ++c) xaddr[c] = lds0 + rg * 16384 + n * 512 + (((2 * c + h) ^ (n & 15)) << 4);
  auto xfrag = [&](int job, int ks) { return lds_read16(xaddr[ks & 7] + ((job + 2) & 3) * 32768 + (ks >> 3) * 256); };
  const u32 tb = lds0 + TB + (w >> 1) * 8192;
  const int cpar = w & 1;
  const u32 tput = tb + n * 128, tsw = (4 * cpar + 2 * h) ^ (n & 7);
  const u32 tget = tb + (16 * cpar + (lane >> 3)) * 128 + (((lane & 7) ^ (lane >> 3)) << 4);
  const u32 slane = (u32)((lane >> 3) * 256 + (lane & 7) * 16);
  f32x16 accs[2];
  bf16x8 xf[8];
#pragma unroll
  for (int ks = 0; ks < 4; ++ks) xf[ks] = xfrag(0, ks);

  auto job_body = [&](auto jc) {
    constexpr int job = decltype(jc)::value;
    f32x16& acc = accs[job & 1];
    const f32x16& accp = accs[(job + 1) & 1];
    u32x4 c0, c1, o[2];
    constexpr int young = job <= 2 ? 4 : job == 3 ? 6 : job == 4 ? 8 : 4;
    if (job <= 5) asm volatile("s_waitcnt vmcnt(%0)" ::"n"(young) : "memory");
    wg_barrier();
    const u32 xs = x_soff(job + 3), xl = x_lds(job + 3);
    const u32 ds = (u32)((rp0 + 24 * (job - 2)) * 64 + rg * 32 + 16 * cpar) * 256u;
    auto gap = [&](auto kc) {
      constexpr int ks = decltype(kc)::value;
      if (job < 7) {
        if (ks == 0) {
          const f32x16 zero = {0};
          acc = mfma(wf[0], xf[0], zero);
        } else {
          acc = mfma(wf[ks], xf[ks & 7], acc);
        }
        asm volatile("" : "+v"(acc));
        __builtin_amdgcn_sched_barrier(0);
        if (ks < 12) xf[(ks + 4) & 7] = xfrag(job, ks + 4);
        else if (job < 6) xf[(ks + 4) & 7] = xfrag(job + 1, ks - 12);
      }
      if (job >= 1 && job <= 7) {
        if (ks >= 1 && ks <= 4) {
          constexpr int i = ks - 1;
          c0[i] = pack_bf16(accp[2 * i] * scale, accp[2 * i + 1] * scale);
          c1[i] = pack_bf16(accp[8 + 2 * i] * scale, accp[8 + 2 * i + 1] * scale);
        }
        if (ks == 5) {
          const u32 t = tput + ((job - 1) & 1) * 4096;
          *(LDSP u32x4*)(size_t)(t + (tsw << 4)) = c0;
          *(LDSP u32x4*)(size_t)(t + ((tsw ^ 1) << 4)) = c1;
        }
      }
      if (job >= 2 && ks == 0) {
#pragma unroll
        for (int i = 0; i < 2; ++i) o[i] = lds_read16u(tget + ((job - 2) & 1) * 4096 + i * 1024);
      }
      if (job <= 3 && ks >= XK0 && ks <= XK0 + 3)
        bdma16(xr, (xlane ^ (u32)((ks - XK0) << 5)) + (ks - XK0) * 1024, xs, xl + (ks - XK0) * 1024);
      if (job >= 2 && (ks == 10 || ks == 11)) {
        __builtin_amdgcn_raw_buffer_store_b128(o[ks & 1], dr, slane + (ks & 1) * 2048, ds, TBL_AUX);
      }
      __builtin_amdgcn_sched_barrier(0);
    };
    gap(IC<0>{}); gap(IC<1>{}); gap(IC<2>{}); gap(IC<3>{}); gap(IC<4>{}); gap(IC<5>{}); gap(IC<6>{}); gap(IC<7>{});
    gap(IC<8>{}); gap(IC<9>{}); gap(IC<10>{}); gap(IC<11>{}); gap(IC<12>{}); gap(IC<13>{}); gap(IC<14>{});
    gap(IC<15>{});
    if (job == 0) { LSTAMP(ws, 0, 4); LCYC(ws, 0, 8); }
    if (job == 3) LSTAMP(ws, 0, 5);
    if (job == 6) { LSTAMP(ws, 0, 6); LCYC(ws, 0, 9); }
  };
  job_body(IC<0>{}); job_body(IC<1>{}); job_body(IC<2>{}); job_body(IC<3>{}); job_body(IC<4>{});
  job_body(IC<5>{}); job_body(IC<6>{});
  {
    const u32 d6 = lds0 + 98304 + (w >> 1) * 4096 - tb;
    const f32x16& al = accs[0];
    u32x4 c0, c1, o[4];
#pragma unroll
    for (int i = 0; i < 4; ++i) {
      c0[i] = pack_bf16(al[2 * i] * scale, al[2 * i + 1] * scale);
      c1[i] = pack_bf16(al[8 + 2 * i] * scale, al[8 + 2 * i + 1] * scale);
    }
    *(LDSP u32x4*)(size_t)(tput + d6 + (tsw << 4)) = c0;
    *(LDSP u32x4*)(size_t)(tput + d6 + ((tsw ^ 1) << 4)) = c1;
    wg_barrier();
#pragma unroll
    for (int i = 0; i < 2; ++i) {
      o[i] = lds_read16u(tget + 4096 + i * 1024);
      o[2 + i] = lds_read16u(tget + d6 + i * 1024);
    }
    const u32 ds5 = (u32)((rp0 + 24 * 5) * 64 + rg * 32 + 16 * cpar) * 256u;
    const u32 ds6 = (u32)((rp0 + 24 * 6) * 64 + rg * 32 + 16 * cpar) * 256u;
#pragma unroll
    for (int i = 0; i < 2; ++i) {
      __builtin_amdgcn_raw_buffer_store_b128(o[i], dr, slane + i * 2048, ds5, TBL_AUX);
      __builtin_amdgcn_raw_buffer_store_b128(o[2 + i], dr, slane + i * 2048, ds6, TBL_AUX);
    }
  }
}

__global__ void __launch_bounds__(512, 2)
prep_kernel(const char* __restrict__ x, const char* __restrict__ eattr, const int* __restrict__ src,
            const int* __restrict__ dst, const char* __restrict__ uw, const char* __restrict__ vw,
            const char* __restrict__ gw, char* __restrict__ ws) {
  extern __shared__ __attribute__((aligned(16))) char smem[];
  const int b = blockIdx.x;
  LSTAMP(ws, 0, 0);
  if (b < NPROD)
    producer_block(smem, b, src, dst, eattr, gw, ws);
  else
    gemm_block(smem, b - NPROD, x, uw, vw, gw, ws);
  LSTAMP(ws, 0, 1);
}

constexpr u32 L_QV = 0;
constexpr u32 L_E = 65536;
constexpr u32 L_S = L_E + 8 * 4096;
constexpr u32 L_A1 = L_S + 8 * 2048 + 128;
constexpr int LDUMMY = TPG * CAPT;
constexpr u32 L_A2 = L_A1 + (LDUMMY + 16) * 4;
constexpr u32 L_DL = L_A2 + (LDUMMY + 16) * 8;
constexpr u32 L_GS = L_DL + 8 * 64 * 8;
constexpr u32 L_GE = L_GS + 256;
constexpr u32 L_TT = L_GE + 256;
constexpr u32 L_PROG = L_TT + 64;
constexpr u32 L_JUNK = L_PROG + 64;
constexpr u32 K2_LDS = L_JUNK + 256;
static_assert(K2_LDS <= 163840, "LDS budget");

#define SB() __builtin_amdgcn_sched_barrier(0)
__device__ __forceinline__ void mfma_acc(f32x16& c, bf16x8 a, bf16x8 b) {
  c = mfma(a, b, c);
  asm volatile("" : "+v"(c));
  SB();
}
__device__ __forceinline__ void mfma_new(f32x16& c, bf16x8 a, bf16x8 b) {
  const f32x16 zero = {0};
  c = mfma(a, b, zero);
  asm volatile("" : "+v"(c));
  SB();
}
__device__ __forceinline__ void arrived4(const bf16x8 (&v)[4]) {
  asm volatile("" ::"v"(v[0]), "v"(v[1]), "v"(v[2]), "v"(v[3]) : "memory");
}
__device__ __forceinline__ void dma16x4(const void* sbase, const u32 (&vo)[4], u32 lds) {
  asm volatile(
      "s_mov_b32 m0, %5\n\ts_nop 0\n\t"
      "global_load_lds_dwordx4 %0, %4\n\t"
      "global_load_lds_dwordx4 %1, %4 offset:1024\n\t"
      "global_load_lds_dwordx4 %2, %4 offset:2048\n\t"
      "global_load_lds_dwordx4 %3, %4 offset:3072"
      :
      : "v"(vo[0]), "v"(vo[1]), "v"(vo[2]), "v"(vo[3]), "s"(sbase), "s"(lds)
      : "memory");
}
template <int A, int B>
__device__ __forceinline__ void exp_range(f32x16& d, const f32x16& s) {
#pragma unroll
  for (int i = A; i < B; ++i) d[i] = __builtin_amdgcn_exp2f(s[i]);
}
template <int A, int B>
__device__ __forceinline__ void rcp_range(f32x16& d) {
#pragma unroll
  for (int i = A; i < B; ++i) d[i] = __builtin_amdgcn_rcpf(d[i]);
}
__device__ __forceinline__ void mul16(f32x16& g, const f32x16& v) {
#pragma unroll
  for (int i = 0; i < 8; ++i) {
    const f32x2 a = (f32x2){g[2 * i], g[2 * i + 1]} * (f32x2){v[2 * i], v[2 * i + 1]};
    g[2 * i] = a.x;
    g[2 * i + 1] = a.y;
  }
  asm volatile("" : "+v"(g));
}
__device__ __forceinline__ void cvt16(u32x4& m0, u32x4& m1, const f32x16& d) {
#pragma unroll
  for (int i = 0; i < 4; ++i) {
    m0[i] = pack_bf16(d[2 * i], d[2 * i + 1]);
    m1[i] = pack_bf16(d[8 + 2 * i], d[8 + 2 * i + 1]);
  }
  asm volatile("" : "+v"(m0), "+v"(m1));
}
template <int NV>
__device__ __forceinline__ void wait_vm() { asm volatile("s_waitcnt vmcnt(%0)" ::"n"(NV) : "memory"); }
template <int NV>
__device__ __forceinline__ void wait_lgkm() { asm volatile("s_waitcnt lgkmcnt(%0)" ::"n"(NV) : "memory"); }

__global__ void __launch_bounds__(512, 2)
edge_kernel(const char* __restrict__ ws, u16* __restrict__ out) {
  extern __shared__ __attribute__((aligned(16))) char smem[];
  const int tid = threadIdx.x, lane = tid & 63;
  const int w = __builtin_amdgcn_readfirstlane(tid >> 6);
  const u32 lds0 = (u32)(size_t)(LDSP char*)smem;
  const int b = blockIdx.x, xcd = b & 7;
  const int slice = xcd & 3, group = (b >> 3) * 2 + (xcd >> 2);
  const int h = lane >> 5, f = lane & 31;
  STAMP(ws, 1, 0);
  LSTAMP(ws, 1, 0);

  const int pbk = tid >> 3, pj = tid & 7;
  u32 rec0[8], o0, o1;
  u32x4 cwr[2];
  {
    const char* sbase = ws + WS_SLOT + (size_t)group * NPROD * CAPG * 4;
    const u32 so = (u32)(pbk * CAPG + pj) * 4u;
    asm volatile(
        "global_load_dword %0, %8, %9\n\tglobal_load_dword %1, %8, %9 offset:32\n\t"
        "global_load_dword %2, %8, %9 offset:64\n\tglobal_load_dword %3, %8, %9 offset:96\n\t"
        "global_load_dword %4, %8, %9 offset:128\n\tglobal_load_dword %5, %8, %9 offset:160\n\t"
        "global_load_dword %6, %8, %9 offset:192\n\tglobal_load_dword %7, %8, %9 offset:224"
        : "=&v"(rec0[0]), "=&v"(rec0[1]), "=&v"(rec0[2]), "=&v"(rec0[3]), "=&v"(rec0[4]), "=&v"(rec0[5]),
          "=&v"(rec0[6]), "=&v"(rec0[7])
        : "v"(so), "s"(sbase)
        : "memory");
    const char* obase = ws + WS_OFF + (group * TPG + w) * 2;
    const u32 oo = (u32)lane * (OFFP * 2);
    asm volatile("global_load_ushort %0, %2, %3\n\tglobal_load_ushort %1, %2, %3 offset:2"
                 : "=&v"(o0), "=&v"(o1)
                 : "v"(oo), "s"(obase)
                 : "memory");
    const char* cbase = ws + WS_CWF + slice * 2048;
    const u32 co = (u32)lane * 16u;
    asm volatile("global_load_dwordx4 %0, %2, %3\n\tglobal_load_dwordx4 %1, %2, %3 offset:1024"
                 : "=&v"(cwr[0]), "=&v"(cwr[1])
                 : "v"(co), "s"(cbase)
                 : "memory");
  }
  {
    const u32x4 z = {0, 0, 0, 0};
    *(LDSP u32x4*)(size_t)(lds0 + L_S + w * 2048 + lane * 16) = z;
    *(LDSP u32x4*)(size_t)(lds0 + L_S + w * 2048 + 1024 + lane * 16) = z;
  }
  bf16x8 cw[2], sel[2], one;
#pragma unroll
  for (int s = 0; s < 2; ++s)
#pragma unroll
    for (int j = 0; j < 8; ++j) sel[s][j] = (__bf16)(f == 16 * s + 8 * h + j ? 1.0f : 0.0f);
#pragma unroll
  for (int j = 0; j < 8; ++j) one[j] = (__bf16)((j == 0 && h == 0) ? 1.0f : 0.0f);
  u32 cq[4];
#pragma unroll
  for (int j = 0; j < 4; ++j) cq[j] = (u32)(((lane & 7) ^ (4 * (j & 1) + (lane >> 4))) * 16 + 3072 - 1024 * j);
  u32 ce = (u32)(((lane & 1) ^ ((lane >> 4) & 1)) * 16);
  u32 qaddr[4], saddr[2], staddr[2], eaddr, a1, a2, paddr;
  {
    const int sr = (f & 0x13) | ((f & 4) << 1) | ((f & 8) >> 1);
    const int sq = ((sr & 7) << 2) | (sr >> 3);
    const int fsw = (sr >> 1) & 7;
    eaddr = lds0 + L_E + sq * 32 + ((h ^ ((sq >> 3) & 1)) << 4);
#pragma unroll
    for (int c = 0; c < 4; ++c) qaddr[c] = lds0 + L_QV + sr * 128 + (((2 * c + h) ^ fsw) << 4);
    const int blk = (lane >> 4) & 1, qq = (lane & 15) >> 2, p = lane & 3;
    const int c0 = 16 * blk + 8 * (p & 1) + 4 * (p >> 1);
#pragma unroll
    for (int s = 0; s < 2; ++s) {
      saddr[s] = lds0 + L_S + f * 64 + (((2 * s + h) ^ ((f >> 2) & 3)) << 4);
      const int nu = 8 * s + 4 * h + qq;
      staddr[s] = lds0 + L_S + nu * 64 + (((c0 >> 3) ^ ((nu >> 2) & 3)) << 4) + ((c0 & 7) << 1);
    }
    a1 = lds0 + L_A1 + (lane >> 3) * 16;
    a2 = lds0 + L_A2 + (lane >> 1) * 8;
    paddr = lds0 + L_QV + (4 * h + qq) * 128 + (16 * blk + 4 * p) * 2;
  }
  asm volatile("" : "+v"(sel[0]), "+v"(sel[1]), "+v"(one), "+v"(cq[0]), "+v"(cq[1]), "+v"(cq[2]), "+v"(cq[3]), "+v"(ce));
  asm volatile("" : "+v"(qaddr[0]), "+v"(qaddr[1]), "+v"(qaddr[2]), "+v"(qaddr[3]), "+v"(saddr[0]), "+v"(saddr[1]),
               "+v"(staddr[0]), "+v"(staddr[1]), "+v"(eaddr), "+v"(a1), "+v"(a2), "+v"(paddr));
  SB();
  asm volatile("s_waitcnt vmcnt(0)"
               : "+v"(rec0[0]), "+v"(rec0[1]), "+v"(rec0[2]), "+v"(rec0[3]), "+v"(rec0[4]), "+v"(rec0[5]),
                 "+v"(rec0[6]), "+v"(rec0[7]), "+v"(o0), "+v"(o1), "+v"(cwr[0]), "+v"(cwr[1])
               :
               : "memory");
  SB();
  LSTAMP(ws, 1, 11);
  {
    const int tile = group * TPG + w;
    const int n0w = tile_start(tile), tszw = tile_start(tile + 1) - n0w;
    const char* pu = ws + WS_PU + (size_t)slice * SLICEB;
    const u32 rb = lds0 + L_QV + w * 8192;
#pragma unroll
    for (int j = 0; j < 4; ++j) {
      const int row = 8 * j + (lane >> 3);
      const int node = n0w + (row < tszw ? row : tszw - 1);
      const u32 voff = (u32)node * 256u + (lane & 7) * 16;
      dma16(pu, voff, rb + j * 1024);
    }
    constexpr int ROWS = NROWP / 32;
    const int idx = w * 64 + lane;
    const u32 row = (u32)(b >> 3) * ROWS + (u32)(idx < ROWS ? idx : ROWS - 1);
    asm volatile("s_mov_b32 m0, %2\n\ts_nop 0\n\tglobal_load_lds_dword %0, %1"
                 :
                 : "v"(row * 256u), "s"(ws + WS_QV + (size_t)slice * SLICEB), "s"(lds0 + L_JUNK)
                 : "memory");
  }
  cw[0] = __builtin_bit_cast(bf16x8, cwr[0]);
  cw[1] = __builtin_bit_cast(bf16x8, cwr[1]);
  const int cnt = (int)o1 - (int)o0;
  const int inc = (int)wave_scan((u32)cnt);
  u32x2* dl = (u32x2*)(smem + L_DL);
  {
    const int delta = inc - cnt - (int)o0;
    dl[w * 64 + lane] = (u32x2){(u32)(delta + w * CAPT), (u32)min((int)o1, CAPT - delta)};
  }
  if (w == 0) ((int*)(smem + L_GS))[lane] = (int)o0;
  if (w == 7) ((int*)(smem + L_GE))[lane] = (int)o1;
  if (lane == 63) ((int*)(smem + L_TT))[w] = inc;
  STAMP(ws, 1, 1);
  wg_barrier();
  STAMP(ws, 1, 2);
  LSTAMP(ws, 1, 6);

  const int* tt = (const int*)(smem + L_TT);
  int tau, mytot, maxt;
  {
    const int ti = tt[lane >> 3], tj = tt[lane & 7];
    const bool before = ti > tj || (ti == tj && (lane >> 3) < (lane & 7));
    const unsigned long long m = __ballot(before);
    const int rank = __popcll(m & (0x0101010101010101ull << (lane & 7)));
    const int want = w < 4 ? w : 11 - w;
    tau = __builtin_ctzll(__ballot(lane < 8 && rank == want));
    mytot = __builtin_amdgcn_readlane(tj, tau);
    maxt = __builtin_amdgcn_readlane(tj, __builtin_ctzll(__ballot(lane < 8 && rank == 0)));
  }
  const int npass = maxt > CAPT ? (maxt + CAPT - 1) / CAPT : 1;
  const int tile = group * TPG + tau;
  const int n0 = tile_start(tile), tsz = tile_start(tile + 1) - n0;
  const u32 qb = lds0 + L_QV + tau * 8192, vb = qb + 4096;
  const u32* grec = (const u32*)(ws + WS_REC);

  const char* qsrc = ws + WS_QV + (size_t)slice * SLICEB - 3072;
  const char* vsrc = qsrc + 128;
  const char* esrc = ws + WS_EP;
  const u32 eb = lds0 + L_E + tau * 4096;
  const u32 simg = lds0 + L_S;
  eaddr += tau * 4096;
#pragma unroll
  for (int c = 0; c < 4; ++c) qaddr[c] += tau * 8192;
#pragma unroll
  for (int s = 0; s < 2; ++s) {
    saddr[s] += tau * 2048;
    staddr[s] += tau * 2048;
  }
  a1 += tau * (CAPT * 4);
  a2 += tau * (CAPT * 8);
  paddr += tau * 8192;
  auto read_st = [&](int s) { return join8(lds_read_tr(staddr[0] + s * 1024), lds_read_tr(staddr[1] + s * 1024)); };
  STAMP(ws, 1, 3);
  LSTAMP(ws, 1, 7);

  auto build_lists = [&](int pass, bool first) {
    u32* A1 = (u32*)(smem + L_A1);
    u32x2* A2 = (u32x2*)(smem + L_A2);
    const int tb = threadIdx.x >> 3, tj = threadIdx.x & 7;
    const int bs0 = ((const int*)(smem + L_GS))[tb], be0 = ((const int*)(smem + L_GE))[tb];
    const u32x2* dlb = dl + tb;
    const int pos0 = tb * CHUNK;
    auto put = [&](u32 rec, int i, u32 li, bool ok) {
      const u32 idx = ok ? li : (u32)(LDUMMY + (threadIdx.x & 15));
      A1[idx] = rec & 0x3FFF00u;
      const u32 sa = ((rec >> 18) & 0x3FF0u) ^ (((li & 3) << 4) | ((li >> 1) & 0xE));
      A2[idx] = (u32x2){(u32)(pos0 + i) << 5, sa};
    };
    int i0 = bs0 + tj;
    if (first) {
      u32x2 d[8];
#pragma unroll
      for (int k = 0; k < 6; ++k) d[k] = dlb[(rec0[k] >> 29) * 64];
#pragma unroll
      for (int k = 0; k < 6; ++k) {
        const int i = i0 + 8 * k;
        put(rec0[k], i, (u32)i + d[k].x, i < (int)d[k].y);
      }
#pragma unroll
      for (int k = 6; k < 8; ++k) {
        const int i = i0 + 8 * k;
        if (__any(i < be0)) {
          const u32x2 dk = dlb[(rec0[k] >> 29) * 64];
          put(rec0[k], i, (u32)i + dk.x, i < (int)dk.y);
        }
      }
      i0 += CAPG;
    }
    for (int i = i0; i < be0; i += 8) {
      const u32 rec = grec[pos0 + i];
      const u32 t = rec >> 29;
      const u32 rel = (u32)i + dlb[t * 64].x - t * CAPT - (u32)(pass * CAPT);
      put(rec, i, t * CAPT + rel, rel < (u32)CAPT);
    }
    int tw = ((const int*)(smem + L_TT))[w] - pass * CAPT;
    tw = tw < 0 ? 0 : tw;
    const int ln = threadIdx.x & 63;
    if (tw < CAPT && ln < 32) {
      const int li = (tw & ~31) + ln;
      if (li >= tw) {
        A1[w * CAPT + li] = (u32)N * 256u;
        A2[w * CAPT + li] = (u32x2){(u32)E * 32u, 8u * 2048u};
      }
    }
  };
  build_lists(0, true);
  LSTAMP(ws, 1, 12);
  wait_vm<1>();
  LSTAMP(ws, 1, 13);
  STAMP(ws, 1, 4);
  wg_barrier();
  bf16x8 pb[2][2];
#pragma unroll
  for (int ct = 0; ct < 2; ++ct)
#pragma unroll
    for (int s = 0; s < 2; ++s) {
      const u32 a = paddr + s * 2048 + ct * 64;
      pb[ct][s] = join8(lds_read_tr(a), lds_read_tr(a + 8 * 128));
    }
  f32x16 agg[2] = {{0}, {0}};
  auto u_offsets = [&](u32 (&o)[4]) {
    const int ln = fresh_lane();
#pragma unroll
    for (int j = 0; j < 4; ++j) {
      const int row = 8 * j + (ln >> 3);
      o[j] = (u32)(WS_PU - WS_QV) + (u32)(n0 + (row < tsz ? row : tsz - 1)) * 256u + cq[j];
    }
  };

  for (int pass = 0; pass < npass; ++pass) {
    if (pass > 0) {
      build_lists(pass, false);
      wg_barrier();
    }
    STAMP(ws, 1, 5);
    LSTAMP(ws, 1, 1);

    int rem = mytot - pass * CAPT;
    rem = rem < 0 ? 0 : (rem > CAPT ? CAPT : rem);
    const int nsteps = __builtin_amdgcn_readfirstlane(rem > 0 ? (rem + 31) >> 5 : 1);
    {
      const int last = nsteps - 1;
      auto clampt = [&](int t) { return t < last ? t : last; };
      auto rd16 = [&](int t) -> u32 { return *(const LDSP u32*)(size_t)(a2 + t * 256 + 4); };
      auto rd32 = [&](int t) -> u32 { return *(const LDSP u32*)(size_t)(a2 + t * 256); };
      auto s_write = [&](u32 a, u16 v) { *(LDSP u16*)(size_t)(simg + a) = v; };
      auto dma_e = [&](u32 eo, int t) { dma16(esrc, eo + ce, eb + (t & 3) * 1024); };
      u32 vo[4];
      auto set_vo = [&](const u32x4& so) {
#pragma unroll
        for (int j = 0; j < 4; ++j) vo[j] = so[j] + cq[j];
      };
      const f32x16 zero = {0};

      __builtin_amdgcn_s_setprio(3);
      const u32x4 so0 = lds_read16u(a1);
      const u32 eo0 = rd32(0), eo1 = rd32(clampt(1)), eo2 = rd32(clampt(2)), eo3 = rd32(clampt(3));
      u32 sa = rd16(0);
      u32 sa1 = rd16(clampt(1));
      const u32x4 so1 = lds_read16u(a1 + clampt(1) * 128);
      asm volatile("" ::"v"(pb[0][0]), "v"(pb[0][1]), "v"(pb[1][0]), "v"(pb[1][1]) : "memory");
      SB();
      set_vo(so0);
      dma_e(eo0, 0);
      dma16x4(qsrc, vo, qb);
      dma16x4(vsrc, vo, vb);
      dma_e(eo1, 1);
      dma_e(eo2, 2);
      s_write(sa, (u16)0x3F80);
      mem_fence();
      bf16x8 stf[2], sf[2], qf[4], vfr[4], ef;
#pragma unroll
      for (int s = 0; s < 2; ++s) stf[s] = read_st(s);
      LSTAMP(ws, 1, 14);
      wait_vm<6>();
      LSTAMP(ws, 1, 15);
      ef = lds_read16(eaddr);
#pragma unroll
      for (int c = 0; c < 4; ++c) qf[c] = lds_read16(qaddr[c]);
      set_vo(so1);
      arrived4(qf);
      dma16x4(qsrc, vo, qb);
      f32x16 za, zb, ea, ebv = zero, vf;
      za = mfma(ef, cw[0], zero);
      zb = mfma(ef, cw[1], zero);
      za = mfma(stf[0], pb[0][0], za);
      zb = mfma(stf[0], pb[1][0], zb);
      za = mfma(stf[1], pb[0][1], za);
      zb = mfma(stf[1], pb[1][1], zb);
      za = mfma(qf[0], sel[0], za);
      zb = mfma(qf[2], sel[0], zb);
      za = mfma(qf[1], sel[1], za);
      u32x4 m0 = {0, 0, 0, 0}, m1 = {0, 0, 0, 0};
      sf[0] = __builtin_bit_cast(bf16x8, m0);
      sf[1] = sf[0];
      dma_e(eo3, 3);
      SB();

      LSTAMP(ws, 1, 4);
      LCYC(ws, 1, 9);
      __builtin_amdgcn_s_setprio(1);
      for (int t = 0; t <= last; ++t) {
        wait_vm<5>();
#pragma unroll
        for (int c = 0; c < 4; ++c) vfr[c] = lds_read16(qaddr[c] + 4096);
        SB();
        const u32x4 so = lds_read16u(a1 + clampt(t + 2) * 128);
        const u32 eo = rd32(clampt(t + 4));
        const u32 sa2 = rd16(clampt(t + 2));
        SB();
        mfma_acc(zb, qf[3], sel[1]);
        cvt16(m0, m1, ebv);
        SB();
        mfma_acc(agg[1], sf[0], __builtin_bit_cast(bf16x8, m0));
        exp_range<0, 4>(ea, za);
        SB();
        mfma_acc(agg[1], sf[1], __builtin_bit_cast(bf16x8, m1));
        sf[0] = lds_read16(saddr[0]);
        sf[1] = lds_read16(saddr[1]);
        mem_fence();
        s_write(sa, (u16)0);
        s_write(sa1, (u16)0x3F80);
        mem_fence();
        stf[0] = read_st(0);
        stf[1] = read_st(1);
        exp_range<4, 8>(ea, za);
        SB();
        mfma_new(vf, vfr[0], sel[0]);
        ef = lds_read16(eaddr + ((t + 1) & 3) * 1024);
        exp_range<8, 12>(ea, za);
        SB();
        mfma_acc(vf, vfr[1], sel[1]);
        arrived4(vfr);
        if (t == last && pass + 1 == npass) u_offsets(vo);
        dma16x4(vsrc, vo, vb);
        wait_vm<5>();
#pragma unroll
        for (int c = 0; c < 4; ++c) qf[c] = lds_read16(qaddr[c]);
        sa = sa1;
        sa1 = sa2;
        exp_range<12, 16>(ea, za);
        SB();
        __builtin_amdgcn_s_setprio(0);
        set_vo(so);
        exp_range<0, 2>(ebv, zb);
        SB();
        mfma_acc(ea, one, one);
        exp_range<2, 5>(ebv, zb);
        SB();
        mfma_new(za, ef, cw[0]);
        exp_range<5, 8>(ebv, zb);
        SB();
        mfma_acc(za, stf[0], pb[0][0]);
        arrived4(qf);
        dma16x4(qsrc, vo, qb);
        dma_e(eo, t + 4);
        exp_range<8, 11>(ebv, zb);
        SB();
        mfma_acc(za, stf[1], pb[0][1]);
        exp_range<11, 14>(ebv, zb);
        SB();
        mfma_acc(za, qf[0], sel[0]);
        exp_range<14, 16>(ebv, zb);
        SB();
        rcp_range<0, 1>(ea);
        SB();
        mfma_acc(ebv, one, one);
        rcp_range<1, 4>(ea);
        SB();
        mfma_acc(za, qf[1], sel[1]);
        rcp_range<4, 7>(ea);
        SB();
        mfma_new(zb, ef, cw[1]);
        rcp_range<7, 10>(ea);
        SB();
        mfma_acc(zb, stf[0], pb[1][0]);
        rcp_range<10, 13>(ea);
        SB();
        mfma_acc(zb, stf[1], pb[1][1]);
        rcp_range<13, 16>(ea);
        SB();
        mul16(ea, vf);
        SB();
        __builtin_amdgcn_s_setprio(1);
        mfma_new(vf, vfr[2], sel[0]);
        cvt16(m0, m1, ea);
        SB();
        mfma_acc(vf, vfr[3], sel[1]);
        rcp_range<0, 4>(ebv);
        SB();
        mfma_acc(agg[0], sf[0], __builtin_bit_cast(bf16x8, m0));
        rcp_range<4, 8>(ebv);
        SB();
        mfma_acc(agg[0], sf[1], __builtin_bit_cast(bf16x8, m1));
        rcp_range<8, 12>(ebv);
        SB();
        mfma_acc(zb, qf[2], sel[0]);
        rcp_range<12, 16>(ebv);
        SB();
        mul16(ebv, vf);
        SB();
      }
      __builtin_amdgcn_s_setprio(0);
      cvt16(m0, m1, ebv);
      LSTAMP(ws, 1, 5);
      LCYC(ws, 1, 10);
      agg[1] = mfma(sf[0], __builtin_bit_cast(bf16x8, m0), agg[1]);
      agg[1] = mfma(sf[1], __builtin_bit_cast(bf16x8, m1), agg[1]);
      s_write(sa, (u16)0);
      wait_vm0();
    }
    if (pass + 1 < npass) wg_barrier();
  }
  STAMP(ws, 1, 6);
  LSTAMP(ws, 1, 2);

  {
    const int lane = fresh_lane(), h = lane >> 5;
    const int blk = (lane >> 4) & 1, qq = (lane & 15) >> 2, p = lane & 3;
#pragma unroll
    for (int ct = 0; ct < 2; ++ct)
#pragma unroll
      for (int s = 0; s < 2; ++s) {
        s16x4 part[2];
#pragma unroll
        for (int t = 0; t < 2; ++t) {
          const int row = 16 * s + 8 * h + 4 * t + qq;
          const int ch = 4 * ct + 2 * blk + (p >> 1);
          part[t] = lds_read_tr(vb + row * 128 + ((ch ^ ((row >> 1) & 7)) << 4) + (p & 1) * 8);
        }
        agg[ct] = mfma(sel[s], join8(part[0], part[1]), agg[ct]);
      }
  }
  {
  const int lane = fresh_lane(), h = lane >> 5, f = lane & 31;
  const u32 obase = qb + 4 * h * 128 + f * 2;
#pragma unroll
  for (int ct = 0; ct < 2; ++ct) {
    f32x2 a[6], g[6];
#pragma unroll
    for (int i = 0; i < 6; ++i) {
      a[i] = (f32x2){agg[ct][2 * i], agg[ct][2 * i + 1]};
      const f32x2 y = a[i] * (f32x2){NLOG2E, NLOG2E};
      const f32x2 d = (f32x2){__builtin_amdgcn_exp2f(y.x), __builtin_amdgcn_exp2f(y.y)} + (f32x2){1.0f, 1.0f};
      g[i] = (f32x2){__builtin_amdgcn_rcpf(d.x), __builtin_amdgcn_rcpf(d.y)};
    }
#pragma unroll
    for (int i = 0; i < 6; ++i) {
      const int r = 2 * i;
      const f32x2 o = a[i] * g[i];
      const u32 pk = pack_bf16(o.x, o.y);
      const u32 ad = obase + ((r & 3) + 8 * (r >> 2)) * 128 + ct * 64;
      *(LDSP u16*)(size_t)ad = (u16)pk;
      *(LDSP u16*)(size_t)(ad + 128) = (u16)(pk >> 16);
    }
  }
  mem_fence();
  {
    const rsrc_t outr = make_rsrc(out, (u32)N * D * 2);
#pragma unroll
    for (int i = 0; i < 3; ++i) {
      const int row = 8 * i + (lane >> 3);
      const u32x4 v = lds_read16u(qb + row * 128 + (lane & 7) * 16);
      if (row < tsz)
        __builtin_amdgcn_raw_buffer_store_b128(v, outr, (u32)(n0 + row) * (D * 2) + slice * 128 + (lane & 7) * 16, 0, OUT_AUX);
    }
  }
  }
  STAMP(ws, 1, 7);
  LSTAMP(ws, 1, 3);
  STAMP(ws, 1, 8);
}

inline void launch_k1(void* const* d_in, void* d_ws, hipStream_t stream) {
  (void)hipFuncSetAttribute((const void*)prep_kernel, hipFuncAttributeMaxDynamicSharedMemorySize, K1_LDS);
  prep_kernel<<<NPROD + NGEMM, 512, K1_LDS, stream>>>((const char*)d_in[0], (const char*)d_in[1], (const int*)d_in[2],
                                                    (const int*)d_in[3], (const char*)d_in[4], (const char*)d_in[5],
                                                    (const char*)d_in[6], (char*)d_ws);
}
inline void launch_k2(void* const* d_in, void* d_out, void* d_ws, hipStream_t stream) {
  constexpr u32 K2_DYN = K2_LDS;
  (void)hipFuncSetAttribute((const void*)edge_kernel, hipFuncAttributeMaxDynamicSharedMemorySize, K2_DYN);
  edge_kernel<<<256, 512, K2_DYN, stream>>>((const char*)d_ws, (u16*)d_out);
}

}

extern "C" void kernel_launch(void* const* d_in, const int* in_sizes, int n_in, void* d_out, int out_size,
                              void* d_ws, size_t ws_size, hipStream_t stream) {
  egc::launch_k1(d_in, d_ws, stream);
  egc::launch_k2(d_in, d_out, d_ws, stream);
}
```

```cpp
#include <hip/hip_runtime.h>
#include <hip/hip_bf16.h>
#include <stdint.h>

namespace egc {

typedef unsigned int u32;
typedef unsigned short u16;
typedef float f32x2 __attribute__((ext_vector_type(2)));
typedef float f32x16 __attribute__((ext_vector_type(16)));
typedef __bf16 bf16x2 __attribute__((ext_vector_type(2)));
typedef __bf16 bf16x4 __attribute__((ext_vector_type(4)));
typedef __bf16 bf16x8 __attribute__((ext_vector_type(8)));
typedef short s16x4 __attribute__((ext_vector_type(4)));
typedef u32 u32x2 __attribute__((ext_vector_type(2)));
typedef u32 u32x4 __attribute__((ext_vector_type(4)));
#define LDSP __attribute__((address_space(3)))

constexpr int N = 10000, D = 256, DE = 16, E = 160000;
constexpr int NPROD = 64;
constexpr int CHUNK = E / NPROD;
constexpr int NTILE = 512;
constexpr int TPG = 8;
constexpr int OFFP = 520;
constexpr int NROWP = 10016;
constexpr size_t SLICEB = (size_t)NROWP * 256;
constexpr int CAPT = 448;
constexpr float NLOG2E = -1.44269504088896340736f;

constexpr size_t WS_QV = 0;
constexpr size_t WS_PU = WS_QV + 4 * SLICEB;
constexpr size_t WS_EP = WS_PU + 4 * SLICEB;
constexpr size_t WS_REC = WS_EP + (size_t)(E + 64) * 32;
constexpr size_t WS_OFF = WS_REC + (size_t)(E + 64) * 4;
constexpr int NGROUP = NTILE / TPG;
constexpr int CAPG = 64;
constexpr size_t WS_SLOT = (WS_OFF + (size_t)NPROD * OFFP * 2 + 255) & ~(size_t)255;
constexpr size_t WS_CWF = WS_SLOT + (size_t)NGROUP * NPROD * CAPG * 4;
constexpr size_t WS_END = WS_CWF + 4 * 2 * 64 * 16;

template <int V>
struct IC {
  static constexpr int value = V;
};

__device__ __forceinline__ int tile_start(int t) { return (625 * t) >> 5; }
__device__ __forceinline__ int tile_of(int d) { return (32 * d + 31) / 625; }

__device__ __forceinline__ f32x16 mfma(bf16x8 a, bf16x8 b, f32x16 c) {
  return __builtin_amdgcn_mfma_f32_32x32x16_bf16(a, b, c, 0, 0, 0);
}
__device__ __forceinline__ u32 pack_bf16(float a, float b) {
  bf16x2 v;
  v[0] = (__bf16)a;
  v[1] = (__bf16)b;
  return __builtin_bit_cast(u32, v);
}
__device__ __forceinline__ float bf16_bits_to_f32(u16 b) { return __builtin_bit_cast(float, (u32)b << 16); }

__device__ __forceinline__ void dma16(const void* sbase, u32 voff, u32 lds) {
  asm volatile("s_mov_b32 m0, %2\n\ts_nop 0\n\tglobal_load_lds_dwordx4 %0, %1"
               :
               : "v"(voff), "s"(sbase), "s"(lds)
               : "memory");
}
typedef __amdgpu_buffer_rsrc_t rsrc_t;

#define XSPREAD 0
#define WARM_MODE 1
#define TBL_AUX 16
#define EP_AUX 16
#define OUT_AUX 16
__device__ __forceinline__ rsrc_t make_rsrc(const void* base, u32 bytes) {
  return __builtin_amdgcn_make_buffer_rsrc((void*)base, 0, bytes, 0x00020000);
}
__device__ __forceinline__ void store16_wt(rsrc_t r, u32 off, u32x4 v) {
  __builtin_amdgcn_raw_buffer_store_b128(v, r, off, 0, 16);
}
__device__ __forceinline__ void store4_wt(rsrc_t r, u32 off, u32 v) {
  __builtin_amdgcn_raw_buffer_store_b32(v, r, off, 0, 16);
}
__device__ __forceinline__ int fresh_lane() {
  u32 z;
  asm volatile("s_mov_b32 %0, 0" : "=s"(z));
  return (int)__builtin_amdgcn_mbcnt_hi(~0u, __builtin_amdgcn_mbcnt_lo(~0u, z));
}
__device__ __forceinline__ void bdma16x4(rsrc_t r, u32 v0, u32 v1, u32 v2, u32 v3, u32 soff, u32 lds) {
  asm volatile(
      "s_mov_b32 m0, %6\n\ts_nop 0\n\t"
      "buffer_load_dwordx4 %0, %4, %5 offen lds\n\t"
      "buffer_load_dwordx4 %1, %4, %5 offen offset:1024 lds\n\t"
      "buffer_load_dwordx4 %2, %4, %5 offen offset:2048 lds\n\t"
      "buffer_load_dwordx4 %3, %4, %5 offen offset:3072 lds"
      :
      : "v"(v0), "v"(v1), "v"(v2), "v"(v3), "s"(r), "s"(soff), "s"(lds)
      : "memory");
}
__device__ __forceinline__ void bdma16(rsrc_t r, u32 v, u32 soff, u32 lds) {
  asm volatile("s_mov_b32 m0, %3\n\ts_nop 0\n\tbuffer_load_dwordx4 %0, %1, %2 offen lds"
               :
               : "v"(v), "s"(r), "s"(soff), "s"(lds)
               : "memory");
}
__device__ __forceinline__ void mem_fence() { asm volatile("" ::: "memory"); }
__device__ __forceinline__ void wait_vm0() { asm volatile("s_waitcnt vmcnt(0)" ::: "memory"); }
__device__ __forceinline__ void wait_lgkm0() { asm volatile("s_waitcnt lgkmcnt(0)" ::: "memory"); }
__device__ __forceinline__ void wg_barrier() {
  asm volatile("s_waitcnt lgkmcnt(0)\n\ts_barrier" ::: "memory");
}
__device__ __forceinline__ bf16x8 lds_read16(u32 addr) { return *(const LDSP bf16x8*)(size_t)addr; }
__device__ __forceinline__ u32x4 lds_read16u(u32 addr) { return *(const LDSP u32x4*)(size_t)addr; }
__device__ __forceinline__ s16x4 lds_read_tr(u32 addr) {
  return __builtin_amdgcn_ds_read_tr16_b64_v4i16((LDSP s16x4*)(size_t)addr);
}
__device__ __forceinline__ bf16x8 join8(s16x4 lo, s16x4 hi) {
  u32x2 a = __builtin_bit_cast(u32x2, lo), b = __builtin_bit_cast(u32x2, hi);
  u32x4 r = {a.x, a.y, b.x, b.y};
  return __builtin_bit_cast(bf16x8, r);
}

#define STAMP(wsp, kid, i)
#define CSTAMP(wsp, kid, i, extra)
#define LSTAMP(wsp, kid, i)
#define LCYC(wsp, kid, i)

constexpr int K1_LDS = 163840;
constexpr int NGEMM = 192;

__device__ __forceinline__ u32 wave_scan(u32 v) {
  v += __builtin_amdgcn_update_dpp(0u, v, 0x111, 0xF, 0xF, false);
  v += __builtin_amdgcn_update_dpp(0u, v, 0x112, 0xF, 0xF, false);
  v += __builtin_amdgcn_update_dpp(0u, v, 0x114, 0xF, 0xF, false);
  v += __builtin_amdgcn_update_dpp(0u, v, 0x118, 0xF, 0xF, false);
  v += __builtin_amdgcn_update_dpp(0u, v, 0x142, 0xA, 0xF, false);
  v += __builtin_amdgcn_update_dpp(0u, v, 0x143, 0xC, 0xF, false);
  return v;
}

__device__ __forceinline__ void producer_block(char* smem, int pb, const int* __restrict__ src,
                                               const int* __restrict__ dst, const char* __restrict__ eattr,
                                               const char* __restrict__ gw, char* __restrict__ ws) {
  u32* hist = (u32*)smem;
  u32* wsum = hist + 512;
  u32* srec = wsum + 16;
  u16* seid = (u16*)(srec + 2560);
  u16* sgrp = seid + 2560;
  constexpr u32 EL = 32768;
  const int tid = threadIdx.x, lane = tid & 63, w = tid >> 6;
  const int e0 = pb * CHUNK;
  const rsrc_t wsr = make_rsrc(ws, (u32)WS_END);
  STAMP(ws, 0, 0);
  int dd[5], ss[5];
#pragma unroll
  for (int k = 0; k < 5; ++k) {
    const int i = tid + 512 * k;
    const int ii = i < CHUNK ? i : 0;
    dd[k] = dst[e0 + ii];
    ss[k] = src[e0 + ii];
  }
  hist[tid] = 0;
  wg_barrier();
  LSTAMP(ws, 0, 2);
  u32 rec[5], tl[5], rk[5];
#pragma unroll
  for (int k = 0; k < 5; ++k) {
    const int i = tid + 512 * k;
    const int t = tile_of(dd[k]);
    const u32 dl = dd[k] - tile_start(t);
    rec[k] = ((u32)ss[k] << 8) | (((dl >> 2) & 3) << 22) | (dl << 24) | ((u32)(t & 7) << 29);
    tl[k] = t;
    rk[k] = 0;
    if (i < CHUNK) rk[k] = atomicAdd(&hist[t], 1u);
  }
  {
    const char* ein = eattr + (size_t)e0 * 32;
    const u32 lds0 = (u32)(size_t)(LDSP char*)smem;
    const int wu = __builtin_amdgcn_readfirstlane(w);
#pragma unroll
    for (int k = 0; k < 10; ++k) {
      const int i = tid + 512 * k;
      dma16(ein, (u32)(i < 2 * CHUNK ? i : 0) * 16u, lds0 + EL + (wu * 64 + 512 * k) * 16);
    }
  }
  wg_barrier();
  const u32 c = hist[tid];
  const u32 inc = wave_scan(c);
  if (lane == 63) wsum[w] = inc;
  wg_barrier();
  u32 wb = 0;
#pragma unroll
  for (int j = 0; j < 8; ++j) wb += j < w ? wsum[j] : 0u;
  const u32 base = wb + inc - c;
  hist[tid] = base;
  u16* off = (u16*)(ws + WS_OFF) + pb * OFFP;
  off[tid] = (u16)base;
  if (tid < OFFP - 512) off[512 + tid] = (u16)CHUNK;
  wg_barrier();
#pragma unroll
  for (int k = 0; k < 5; ++k) {
    const int i = tid + 512 * k;
    if (i < CHUNK) {
      const u32 p = hist[tl[k]] + rk[k];
      srec[p] = rec[k];
      seid[p] = (u16)i;
      sgrp[p] = (u16)(tl[k] >> 3);
    }
  }
  LSTAMP(ws, 0, 3);
  wait_vm0();
  wg_barrier();
  LSTAMP(ws, 0, 4);
#pragma unroll
  for (int k = 0; k < 10; ++k) {
    const int i = tid + 512 * k;
    if (i < 2 * CHUNK)
      __builtin_amdgcn_raw_buffer_store_b128(*(const u32x4*)(smem + EL + (int)seid[i >> 1] * 32 + (i & 1) * 16), wsr,
                                             (u32)WS_EP + (u32)e0 * 32u + i * 16, 0, EP_AUX);
  }
#pragma unroll
  for (int k = 0; k < 5; ++k) {
    const int p = tid + 512 * k;
    if (p < CHUNK) {
      const u32 g = sgrp[p], rel = p - hist[g * 8];
      if (rel < (u32)CAPG) store4_wt(wsr, (u32)WS_SLOT + ((g * NPROD + pb) * CAPG + rel) * 4u, srec[p]);
    }
  }
  if (tid < CHUNK / 4) store16_wt(wsr, (u32)WS_REC + (u32)e0 * 4u + tid * 16, *(const u32x4*)(srec + tid * 4));
  if (tid + 512 < CHUNK / 4)
    store16_wt(wsr, (u32)WS_REC + (u32)e0 * 4u + (tid + 512) * 16, *(const u32x4*)(srec + (tid + 512) * 4));
  STAMP(ws, 0, 3);
  if (pb == 1) {
    const int sl = tid >> 7, ct = (tid >> 6) & 1, hh = lane >> 5, ff = lane & 31;
    u32x4 v;
#pragma unroll
    for (int j = 0; j < 4; ++j) {
      const u16* wrow = (const u16*)gw + (512 + 8 * hh + 2 * j) * D + sl * 64 + ct * 32 + ff;
      v[j] = pack_bf16(bf16_bits_to_f32(wrow[0]) * NLOG2E, bf16_bits_to_f32(wrow[D]) * NLOG2E);
    }
    store16_wt(wsr, (u32)WS_CWF + tid * 16, v);
  }
  if (pb == 0) {
    const u32x4 z = {0, 0, 0, 0};
    if (tid < 64) *(u32x4*)(ws + WS_QV + (tid >> 4) * SLICEB + (size_t)N * 256 + (tid & 15) * 16) = z;
    if (tid >= 64 && tid < 66) *(u32x4*)(ws + WS_EP + (size_t)E * 32 + (tid - 64) * 16) = z;
  }
}

__device__ __forceinline__ void gemm_block(char* smem, int gb, const char* __restrict__ x,
                                           const char* __restrict__ uw, const char* __restrict__ vw,
                                           const char* __restrict__ gw, char* __restrict__ ws) {
  const int tid = threadIdx.x, lane = tid & 63;
  const int w = __builtin_amdgcn_readfirstlane(tid >> 6);
  const u32 lds0 = (u32)(size_t)(LDSP char*)smem;
  const int q = gb >> 3, xcd = gb & 7;
  const int cg = q / 3;
  const int type = cg & 3, half = cg >> 2;
  const int rp0 = (q - 3 * cg) * 8 + xcd;
  const int cw4 = w & 3, rg = w >> 2;
  const char* W = (type == 0 ? gw + 256 * 512 : type == 1 ? vw : type == 2 ? gw : uw) + half * 256;
  const float scale = (type & 1) ? 1.0f : NLOG2E;
  const int ctg = half * 4 + cw4;
  const rsrc_t xr = make_rsrc(x, (u32)N * 512u);
  const rsrc_t dr = make_rsrc(ws + (type < 2 ? WS_QV : WS_PU) + (type & 1) * 128 + (size_t)(ctg >> 1) * SLICEB,
                              (u32)N * 256u - 128u);

  constexpr u32 TB = 131072;
  const u32 xlane = (u32)((lane >> 5) * 512) + ((u32)(((lane & 31) ^ (lane >> 5)) << 4) ^ (u32)((cw4 & 1) << 7));
  auto x_soff = [&](int job) { return (u32)((rp0 + 24 * job) * 64 + rg * 32) * 512u + (u32)cw4 * 4096u; };
  auto x_lds = [&](int job) { return lds0 + ((job + 2) & 3) * 32768 + rg * 16384 + cw4 * 4096; };
  auto load_x = [&](int job) { bdma16x4(xr, xlane, xlane ^ 32u, xlane ^ 64u, xlane ^ 96u, x_soff(job), x_lds(job)); };
  STAMP(ws, 0, 0);
  auto load_w = [&](int q) {
#pragma unroll
    for (int i = 0; i < 2; ++i) {
      const int j = 16 * q + 2 * w + i;
      const int k = 4 * j + (lane >> 4);
      const u32 voff = (u32)k * 512u + (u32)(((lane & 15) ^ ((k & 3) << 2)) * 16);
      dma16(W, voff, lds0 + j * 1024);
    }
  };
  load_w(0);
  load_x(0);
  load_w(1);
  load_w(2);
  load_w(3);
  load_x(1);

  const int h = lane >> 5, blk = (lane >> 4) & 1, qq = (lane & 15) >> 2, p = lane & 3;
  const int n = lane & 31;
  u32 xaddr[8];
#pragma unroll
  for (int c = 0; c < 8; ++c) xaddr[c] = lds0 + rg * 16384 + n * 512 + (((2 * c + h) ^ (n & 15)) << 4);
  auto xfrag = [&](int job, int ks) { return lds_read16(xaddr[ks & 7] + ((job + 2) & 3) * 32768 + (ks >> 3) * 256); };
  const u32 tb = lds0 + TB + (w >> 1) * 8192;
  const int cpar = w & 1;
  const u32 tput = tb + n * 128, tsw = (4 * cpar + 2 * h) ^ (n & 7);
  const u32 tget = tb + (16 * cpar + (lane >> 3)) * 128 + (((lane & 7) ^ (lane >> 3)) << 4);
  const u32 slane = (u32)((lane >> 3) * 256 + (lane & 7) * 16);
  f32x16 accs[2];
  bf16x8 xf[8], wf[16];

  {
    const int col0 = cw4 * 32 + 16 * (p & 1) + 8 * blk + 4 * (p >> 1);
    const u32 a0 = lds0 + (8 * h + qq) * 256 + ((((col0 >> 3) ^ (qq << 2)) << 4) | ((col0 & 7) << 1));
    auto quarter = [&](auto qc, auto nyoung) {
      constexpr int q = decltype(qc)::value;
      asm volatile("s_waitcnt vmcnt(%0)" ::"n"(decltype(nyoung)::value) : "memory");
      wg_barrier();
      if (q == 2) load_x(2);
#pragma unroll
      for (int ks = 4 * q; ks < 4 * q + 4; ++ks) {
        wf[ks] = join8(lds_read_tr(a0 + ks * 4096), lds_read_tr(a0 + ks * 4096 + 1024));
        xf[ks & 7] = xfrag(0, ks);
      }
      if (q == 3) {
#pragma unroll
        for (int ks = 0; ks < 4; ++ks) xf[ks] = xfrag(1, ks);
      }
      __builtin_amdgcn_sched_barrier(0);
#pragma unroll
      for (int ks = 4 * q; ks < 4 * q + 4; ++ks) {
        if (ks == 0) {
          const f32x16 zero = {0};
          accs[0] = mfma(wf[0], xf[0], zero);
        } else {
          accs[0] = mfma(wf[ks], xf[ks & 7], accs[0]);
        }
      }
      asm volatile("" : "+v"(accs[0]));
      __builtin_amdgcn_sched_barrier(0);
    };
    quarter(IC<0>{}, IC<10>{});
    LSTAMP(ws, 0, 2);
    quarter(IC<1>{}, IC<8>{});
    quarter(IC<2>{}, IC<6>{});
    quarter(IC<3>{}, IC<4>{});
    LSTAMP(ws, 0, 4);
    LCYC(ws, 0, 8);
  }

  auto job_body = [&](auto jc) {
    constexpr int job = decltype(jc)::value;
    f32x16& acc = accs[job & 1];
    const f32x16& accp = accs[(job + 1) & 1];
    u32x4 c0, c1, o[2];
    constexpr int young = job == 1 ? 0 : job == 2 ? 4 : job == 3 ? 6 : job == 4 ? 8 : 4;
    if (job <= 5) asm volatile("s_waitcnt vmcnt(%0)" ::"n"(young) : "memory");
    wg_barrier();
    const u32 xs = x_soff(job + 3), xl = x_lds(job + 3);
    const u32 ds = (u32)((rp0 + 24 * (job - 2)) * 64 + rg * 32 + 16 * cpar) * 256u;
    auto gap = [&](auto kc) {
      constexpr int ks = decltype(kc)::value;
      if (job < 7) {
        if (ks == 0) {
          const f32x16 zero = {0};
          acc = mfma(wf[0], xf[0], zero);
        } else {
          acc = mfma(wf[ks], xf[ks & 7], acc);
        }
        asm volatile("" : "+v"(acc));
        __builtin_amdgcn_sched_barrier(0);
        if (ks < 12) xf[(ks + 4) & 7] = xfrag(job, ks + 4);
        else if (job < 6) xf[(ks + 4) & 7] = xfrag(job + 1, ks - 12);
      }
      if (job >= 1 && job <= 7) {
        if (ks >= 1 && ks <= 4) {
          constexpr int i = ks - 1;
          c0[i] = pack_bf16(accp[2 * i] * scale, accp[2 * i + 1] * scale);
          c1[i] = pack_bf16(accp[8 + 2 * i] * scale, accp[8 + 2 * i + 1] * scale);
        }
        if (ks == 5) {
          const u32 t = tput + ((job - 1) & 1) * 4096;
          *(LDSP u32x4*)(size_t)(t + (tsw << 4)) = c0;
          *(LDSP u32x4*)(size_t)(t + ((tsw ^ 1) << 4)) = c1;
        }
      }
      if (job >= 2 && ks == 0) {
#pragma unroll
        for (int i = 0; i < 2; ++i) o[i] = lds_read16u(tget + ((job - 2) & 1) * 4096 + i * 1024);
      }
      if (job == 1 && ks < 4)
        bdma16(xr, (xlane ^ (u32)(ks << 5)) + ks * 1024, x_soff(3), x_lds(3) + ks * 1024);
      constexpr int k0 = job == 1 ? 4 : 0;
      if (job <= 3 && ks >= k0 && ks <= k0 + 3)
        bdma16(xr, (xlane ^ (u32)((ks - k0) << 5)) + (ks - k0) * 1024, xs, xl + (ks - k0) * 1024);
      constexpr int si = ks == 10 ? 0 : ks == 11 ? 1 : -1;
      if (job >= 2 && si >= 0) {
        __builtin_amdgcn_raw_buffer_store_b128(o[si & 1], dr, slane + (si & 1) * 2048, ds, TBL_AUX);
      }
      __builtin_amdgcn_sched_barrier(0);
    };
    gap(IC<0>{}); gap(IC<1>{}); gap(IC<2>{}); gap(IC<3>{}); gap(IC<4>{}); gap(IC<5>{}); gap(IC<6>{}); gap(IC<7>{});
    gap(IC<8>{}); gap(IC<9>{}); gap(IC<10>{}); gap(IC<11>{}); gap(IC<12>{}); gap(IC<13>{}); gap(IC<14>{});
    gap(IC<15>{});
    if (job == 3) LSTAMP(ws, 0, 5);
    if (job == 6) { LSTAMP(ws, 0, 6); LCYC(ws, 0, 9); }
  };
  job_body(IC<1>{}); job_body(IC<2>{}); job_body(IC<3>{}); job_body(IC<4>{}); job_body(IC<5>{}); job_body(IC<6>{});
  {
    const u32 d6 = lds0 + 98304 + (w >> 1) * 4096 - tb;
    const f32x16& al = accs[0];
    u32x4 c0, c1, o[4];
#pragma unroll
    for (int i = 0; i < 4; ++i) {
      c0[i] = pack_bf16(al[2 * i] * scale, al[2 * i + 1] * scale);
      c1[i] = pack_bf16(al[8 + 2 * i] * scale, al[8 + 2 * i + 1] * scale);
    }
    *(LDSP u32x4*)(size_t)(tput + d6 + (tsw << 4)) = c0;
    *(LDSP u32x4*)(size_t)(tput + d6 + ((tsw ^ 1) << 4)) = c1;
    wg_barrier();
#pragma unroll
    for (int i = 0; i < 2; ++i) {
      o[i] = lds_read16u(tget + 4096 + i * 1024);
      o[2 + i] = lds_read16u(tget + d6 + i * 1024);
    }
    const u32 ds5 = (u32)((rp0 + 24 * 5) * 64 + rg * 32 + 16 * cpar) * 256u;
    const u32 ds6 = (u32)((rp0 + 24 * 6) * 64 + rg * 32 + 16 * cpar) * 256u;
#pragma unroll
    for (int i = 0; i < 2; ++i) {
      __builtin_amdgcn_raw_buffer_store_b128(o[i], dr, slane + i * 2048, ds5, TBL_AUX);
      __builtin_amdgcn_raw_buffer_store_b128(o[2 + i], dr, slane + i * 2048, ds6, TBL_AUX);
    }
  }
}

__global__ void __launch_bounds__(512, 2)
prep_kernel(const char* __restrict__ x, const char* __restrict__ eattr, const int* __restrict__ src,
            const int* __restrict__ dst, const char* __restrict__ uw, const char* __restrict__ vw,
            const char* __restrict__ gw, char* __restrict__ ws) {
  extern __shared__ __attribute__((aligned(16))) char smem[];
  const int b = blockIdx.x;
  LSTAMP(ws, 0, 0);
  if (b < NPROD)
    producer_block(smem, b, src, dst, eattr, gw, ws);
  else
    gemm_block(smem, b - NPROD, x, uw, vw, gw, ws);
  LSTAMP(ws, 0, 1);
}

constexpr u32 L_QV = 0;
constexpr u32 L_E = 65536;
constexpr u32 L_S = L_E + 8 * 4096;
constexpr u32 L_A1 = L_S + 8 * 2048 + 128;
constexpr int LDUMMY = TPG * CAPT;
constexpr u32 L_A2 = L_A1 + (LDUMMY + 16) * 4;
constexpr u32 L_DL = L_A2 + (LDUMMY + 16) * 8;
constexpr u32 L_GS = L_DL + 8 * 64 * 8;
constexpr u32 L_GE = L_GS + 256;
constexpr u32 L_TT = L_GE + 256;
constexpr u32 L_PROG = L_TT + 64;
constexpr u32 L_JUNK = L_PROG + 64;
constexpr u32 K2_LDS = L_JUNK + 256;
static_assert(K2_LDS <= 163840, "LDS budget");

#define SB() __builtin_amdgcn_sched_barrier(0)
__device__ __forceinline__ void mfma_acc(f32x16& c, bf16x8 a, bf16x8 b) {
  c = mfma(a, b, c);
  asm volatile("" : "+v"(c));
  SB();
}
__device__ __forceinline__ void mfma_new(f32x16& c, bf16x8 a, bf16x8 b) {
  const f32x16 zero = {0};
  c = mfma(a, b, zero);
  asm volatile("" : "+v"(c));
  SB();
}
__device__ __forceinline__ void arrived4(const bf16x8 (&v)[4]) {
  asm volatile("" ::"v"(v[0]), "v"(v[1]), "v"(v[2]), "v"(v[3]) : "memory");
}
__device__ __forceinline__ void dma16x4(const void* sbase, const u32 (&vo)[4], u32 lds) {
  asm volatile(
      "s_mov_b32 m0, %5\n\ts_nop 0\n\t"
      "global_load_lds_dwordx4 %0, %4\n\t"
      "global_load_lds_dwordx4 %1, %4 offset:1024\n\t"
      "global_load_lds_dwordx4 %2, %4 offset:2048\n\t"
      "global_load_lds_dwordx4 %3, %4 offset:3072"
      :
      : "v"(vo[0]), "v"(vo[1]), "v"(vo[2]), "v"(vo[3]), "s"(sbase), "s"(lds)
      : "memory");
}
template <int A, int B>
__device__ __forceinline__ void exp_range(f32x16& d, const f32x16& s) {
#pragma unroll
  for (int i = A; i < B; ++i) d[i] = __builtin_amdgcn_exp2f(s[i]);
}
template <int A, int B>
__device__ __forceinline__ void rcp_range(f32x16& d) {
#pragma unroll
  for (int i = A; i < B; ++i) d[i] = __builtin_amdgcn_rcpf(d[i]);
}
__device__ __forceinline__ void mul16(f32x16& g, const f32x16& v) {
#pragma unroll
  for (int i = 0; i < 8; ++i) {
    const f32x2 a = (f32x2){g[2 * i], g[2 * i + 1]} * (f32x2){v[2 * i], v[2 * i + 1]};
    g[2 * i] = a.x;
    g[2 * i + 1] = a.y;
  }
  asm volatile("" : "+v"(g));
}
__device__ __forceinline__ void cvt16(u32x4& m0, u32x4& m1, const f32x16& d) {
#pragma unroll
  for (int i = 0; i < 4; ++i) {
    m0[i] = pack_bf16(d[2 * i], d[2 * i + 1]);
    m1[i] = pack_bf16(d[8 + 2 * i], d[8 + 2 * i + 1]);
  }
  asm volatile("" : "+v"(m0), "+v"(m1));
}
template <int NV>
__device__ __forceinline__ void wait_vm() { asm volatile("s_waitcnt vmcnt(%0)" ::"n"(NV) : "memory"); }
template <int NV>
__device__ __forceinline__ void wait_lgkm() { asm volatile("s_waitcnt lgkmcnt(%0)" ::"n"(NV) : "memory"); }

__global__ void __launch_bounds__(512, 2)
edge_kernel(const char* __restrict__ ws, u16* __restrict__ out) {
  extern __shared__ __attribute__((aligned(16))) char smem[];
  const int tid = threadIdx.x, lane = tid & 63;
  const int w = __builtin_amdgcn_readfirstlane(tid >> 6);
  const u32 lds0 = (u32)(size_t)(LDSP char*)smem;
  const int b = blockIdx.x, xcd = b & 7;
  const int slice = xcd & 3, group = (b >> 3) * 2 + (xcd >> 2);
  const int h = lane >> 5, f = lane & 31;
  STAMP(ws, 1, 0);
  LSTAMP(ws, 1, 0);

  const int pbk = tid >> 3, pj = tid & 7;
  u32 rec0[8], o0, o1;
  u32x4 cwr[2];
  {
    const char* sbase = ws + WS_SLOT + (size_t)group * NPROD * CAPG * 4;
    const u32 so = (u32)(pbk * CAPG + pj) * 4u;
    asm volatile(
        "global_load_dword %0, %8, %9\n\tglobal_load_dword %1, %8, %9 offset:32\n\t"
        "global_load_dword %2, %8, %9 offset:64\n\tglobal_load_dword %3, %8, %9 offset:96\n\t"
        "global_load_dword %4, %8, %9 offset:128\n\tglobal_load_dword %5, %8, %9 offset:160\n\t"
        "global_load_dword %6, %8, %9 offset:192\n\tglobal_load_dword %7, %8, %9 offset:224"
        : "=&v"(rec0[0]), "=&v"(rec0[1]), "=&v"(rec0[2]), "=&v"(rec0[3]), "=&v"(rec0[4]), "=&v"(rec0[5]),
          "=&v"(rec0[6]), "=&v"(rec0[7])
        : "v"(so), "s"(sbase)
        : "memory");
    const char* obase = ws + WS_OFF + (group * TPG + w) * 2;
    const u32 oo = (u32)lane * (OFFP * 2);
    asm volatile("global_load_ushort %0, %2, %3\n\tglobal_load_ushort %1, %2, %3 offset:2"
                 : "=&v"(o0), "=&v"(o1)
                 : "v"(oo), "s"(obase)
                 : "memory");
    const char* cbase = ws + WS_CWF + slice * 2048;
    const u32 co = (u32)lane * 16u;
    asm volatile("global_load_dwordx4 %0, %2, %3\n\tglobal_load_dwordx4 %1, %2, %3 offset:1024"
                 : "=&v"(cwr[0]), "=&v"(cwr[1])
                 : "v"(co), "s"(cbase)
                 : "memory");
  }
  {
    const u32x4 z = {0, 0, 0, 0};
    *(LDSP u32x4*)(size_t)(lds0 + L_S + w * 2048 + lane * 16) = z;
    *(LDSP u32x4*)(size_t)(lds0 + L_S + w * 2048 + 1024 + lane * 16) = z;
  }
  bf16x8 cw[2], sel[2], one;
#pragma unroll
  for (int s = 0; s < 2; ++s)
#pragma unroll
    for (int j = 0; j < 8; ++j) sel[s][j] = (__bf16)(f == 16 * s + 8 * h + j ? 1.0f : 0.0f);
#pragma unroll
  for (int j = 0; j < 8; ++j) one[j] = (__bf16)((j == 0 && h == 0) ? 1.0f : 0.0f);
  u32 cq[4];
#pragma unroll
  for (int j = 0; j < 4; ++j) cq[j] = (u32)(((lane & 7) ^ (4 * (j & 1) + (lane >> 4))) * 16 + 3072 - 1024 * j);
  u32 ce = (u32)(((lane & 1) ^ ((lane >> 4) & 1)) * 16);
  u32 qaddr[4], saddr[2], staddr[2], eaddr, a1, a2, paddr;
  {
    const int sr = (f & 0x13) | ((f & 4) << 1) | ((f & 8) >> 1);
    const int sq = ((sr & 7) << 2) | (sr >> 3);
    const int fsw = (sr >> 1) & 7;
    eaddr = lds0 + L_E + sq * 32 + ((h ^ ((sq >> 3) & 1)) << 4);
#pragma unroll
    for (int c = 0; c < 4; ++c) qaddr[c] = lds0 + L_QV + sr * 128 + (((2 * c + h) ^ fsw) << 4);
    const int blk = (lane >> 4) & 1, qq = (lane & 15) >> 2, p = lane & 3;
    const int c0 = 16 * blk + 8 * (p & 1) + 4 * (p >> 1);
#pragma unroll
    for (int s = 0; s < 2; ++s) {
      saddr[s] = lds0 + L_S + f * 64 + (((2 * s + h) ^ ((f >> 2) & 3)) << 4);
      const int nu = 8 * s + 4 * h + qq;
      staddr[s] = lds0 + L_S + nu * 64 + (((c0 >> 3) ^ ((nu >> 2) & 3)) << 4) + ((c0 & 7) << 1);
    }
    a1 = lds0 + L_A1 + (lane >> 3) * 16;
    a2 = lds0 + L_A2 + (lane >> 1) * 8;
    paddr = lds0 + L_QV + (4 * h + qq) * 128 + (16 * blk + 4 * p) * 2;
  }
  asm volatile("" : "+v"(sel[0]), "+v"(sel[1]), "+v"(one), "+v"(cq[0]), "+v"(cq[1]), "+v"(cq[2]), "+v"(cq[3]), "+v"(ce));
  asm volatile("" : "+v"(qaddr[0]), "+v"(qaddr[1]), "+v"(qaddr[2]), "+v"(qaddr[3]), "+v"(saddr[0]), "+v"(saddr[1]),
               "+v"(staddr[0]), "+v"(staddr[1]), "+v"(eaddr), "+v"(a1), "+v"(a2), "+v"(paddr));
  SB();
  asm volatile("s_waitcnt vmcnt(0)"
               : "+v"(rec0[0]), "+v"(rec0[1]), "+v"(rec0[2]), "+v"(rec0[3]), "+v"(rec0[4]), "+v"(rec0[5]),
                 "+v"(rec0[6]), "+v"(rec0[7]), "+v"(o0), "+v"(o1), "+v"(cwr[0]), "+v"(cwr[1])
               :
               : "memory");
  SB();
  LSTAMP(ws, 1, 11);
  {
    const int tile = group * TPG + w;
    const int n0w = tile_start(tile), tszw = tile_start(tile + 1) - n0w;
    const char* pu = ws + WS_PU + (size_t)slice * SLICEB;
    const u32 rb = lds0 + L_QV + w * 8192;
#pragma unroll
    for (int j = 0; j < 4; ++j) {
      const int row = 8 * j + (lane >> 3);
      const int node = n0w + (row < tszw ? row : tszw - 1);
      const u32 voff = (u32)node * 256u + (lane & 7) * 16;
      dma16(pu, voff, rb + j * 1024);
    }
    constexpr int ROWS = NROWP / 32;
    const int idx = w * 64 + lane;
    const u32 row = (u32)(b >> 3) * ROWS + (u32)(idx < ROWS ? idx : ROWS - 1);
    asm volatile("s_mov_b32 m0, %2\n\ts_nop 0\n\tglobal_load_lds_dword %0, %1"
                 :
                 : "v"(row * 256u), "s"(ws + WS_QV + (size_t)slice * SLICEB), "s"(lds0 + L_JUNK)
                 : "memory");
  }
  cw[0] = __builtin_bit_cast(bf16x8, cwr[0]);
  cw[1] = __builtin_bit_cast(bf16x8, cwr[1]);
  const int cnt = (int)o1 - (int)o0;
  const int inc = (int)wave_scan((u32)cnt);
  u32x2* dl = (u32x2*)(smem + L_DL);
  {
    const int delta = inc - cnt - (int)o0;
    dl[w * 64 + lane] = (u32x2){(u32)(delta + w * CAPT), (u32)min((int)o1, CAPT - delta)};
  }
  if (w == 0) ((int*)(smem + L_GS))[lane] = (int)o0;
  if (w == 7) ((int*)(smem + L_GE))[lane] = (int)o1;
  if (lane == 63) ((int*)(smem + L_TT))[w] = inc;
  STAMP(ws, 1, 1);
  wg_barrier();
  STAMP(ws, 1, 2);
  LSTAMP(ws, 1, 6);

  const int* tt = (const int*)(smem + L_TT);
  int tau, mytot, maxt;
  {
    const int ti = tt[lane >> 3], tj = tt[lane & 7];
    const bool before = ti > tj || (ti == tj && (lane >> 3) < (lane & 7));
    const unsigned long long m = __ballot(before);
    const int rank = __popcll(m & (0x0101010101010101ull << (lane & 7)));
    const int want = w < 4 ? w : 11 - w;
    tau = __builtin_ctzll(__ballot(lane < 8 && rank == want));
    mytot = __builtin_amdgcn_readlane(tj, tau);
    maxt = __builtin_amdgcn_readlane(tj, __builtin_ctzll(__ballot(lane < 8 && rank == 0)));
  }
  const int npass = maxt > CAPT ? (maxt + CAPT - 1) / CAPT : 1;
  const int tile = group * TPG + tau;
  const int n0 = tile_start(tile), tsz = tile_start(tile + 1) - n0;
  const u32 qb = lds0 + L_QV + tau * 8192, vb = qb + 4096;
  const u32* grec = (const u32*)(ws + WS_REC);

  const char* qsrc = ws + WS_QV + (size_t)slice * SLICEB - 3072;
  const char* vsrc = qsrc + 128;
  const char* esrc = ws + WS_EP;
  const u32 eb = lds0 + L_E + tau * 4096;
  const u32 simg = lds0 + L_S;
  eaddr += tau * 4096;
#pragma unroll
  for (int c = 0; c < 4; ++c) qaddr[c] += tau * 8192;
#pragma unroll
  for (int s = 0; s < 2; ++s) {
    saddr[s] += tau * 2048;
    staddr[s] += tau * 2048;
  }
  a1 += tau * (CAPT * 4);
  a2 += tau * (CAPT * 8);
  paddr += tau * 8192;
  auto read_st = [&](int s) { return join8(lds_read_tr(staddr[0] + s * 1024), lds_read_tr(staddr[1] + s * 1024)); };
  STAMP(ws, 1, 3);
  LSTAMP(ws, 1, 7);

  auto build_lists = [&](int pass, bool first) {
    u32* A1 = (u32*)(smem + L_A1);
    u32x2* A2 = (u32x2*)(smem + L_A2);
    const int tb = threadIdx.x >> 3, tj = threadIdx.x & 7;
    const int bs0 = ((const int*)(smem + L_GS))[tb], be0 = ((const int*)(smem + L_GE))[tb];
    const u32x2* dlb = dl + tb;
    const int pos0 = tb * CHUNK;
    auto put = [&](u32 rec, int i, u32 li, bool ok) {
      const u32 idx = ok ? li : (u32)(LDUMMY + (threadIdx.x & 15));
      A1[idx] = rec & 0x3FFF00u;
      const u32 sa = ((rec >> 18) & 0x3FF0u) ^ (((li & 3) << 4) | ((li >> 1) & 0xE));
      A2[idx] = (u32x2){(u32)(pos0 + i) << 5, sa};
    };
    int i0 = bs0 + tj;
    if (first) {
      u32x2 d[8];
#pragma unroll
      for (int k = 0; k < 6; ++k) d[k] = dlb[(rec0[k] >> 29) * 64];
#pragma unroll
      for (int k = 0; k < 6; ++k) {
        const int i = i0 + 8 * k;
        put(rec0[k], i, (u32)i + d[k].x, i < (int)d[k].y);
      }
#pragma unroll
      for (int k = 6; k < 8; ++k) {
        const int i = i0 + 8 * k;
        if (__any(i < be0)) {
          const u32x2 dk = dlb[(rec0[k] >> 29) * 64];
          put(rec0[k], i, (u32)i + dk.x, i < (int)dk.y);
        }
      }
      i0 += CAPG;
    }
    for (int i = i0; i < be0; i += 8) {
      const u32 rec = grec[pos0 + i];
      const u32 t = rec >> 29;
      const u32 rel = (u32)i + dlb[t * 64].x - t * CAPT - (u32)(pass * CAPT);
      put(rec, i, t * CAPT + rel, rel < (u32)CAPT);
    }
    int tw = ((const int*)(smem + L_TT))[w] - pass * CAPT;
    tw = tw < 0 ? 0 : tw;
    const int ln = threadIdx.x & 63;
    if (tw < CAPT && ln < 32) {
      const int li = (tw & ~31) + ln;
      if (li >= tw) {
        A1[w * CAPT + li] = (u32)N * 256u;
        A2[w * CAPT + li] = (u32x2){(u32)E * 32u, 8u * 2048u};
      }
    }
  };
  build_lists(0, true);
  LSTAMP(ws, 1, 12);
  wait_vm<1>();
  LSTAMP(ws, 1, 13);
  STAMP(ws, 1, 4);
  wg_barrier();
  bf16x8 pb[2][2];
#pragma unroll
  for (int ct = 0; ct < 2; ++ct)
#pragma unroll
    for (int s = 0; s < 2; ++s) {
      const u32 a = paddr + s * 2048 + ct * 64;
      pb[ct][s] = join8(lds_read_tr(a), lds_read_tr(a + 8 * 128));
    }
  f32x16 agg[2] = {{0}, {0}};
  auto u_offsets = [&](u32 (&o)[4]) {
    const int ln = fresh_lane();
#pragma unroll
    for (int j = 0; j < 4; ++j) {
      const int row = 8 * j + (ln >> 3);
      o[j] = (u32)(WS_PU - WS_QV) + (u32)(n0 + (row < tsz ? row : tsz - 1)) * 256u + cq[j];
    }
  };

  for (int pass = 0; pass < npass; ++pass) {
    if (pass > 0) {
      build_lists(pass, false);
      wg_barrier();
    }
    STAMP(ws, 1, 5);
    LSTAMP(ws, 1, 1);

    int rem = mytot - pass * CAPT;
    rem = rem < 0 ? 0 : (rem > CAPT ? CAPT : rem);
    const int nsteps = __builtin_amdgcn_readfirstlane(rem > 0 ? (rem + 31) >> 5 : 1);
    {
      const int last = nsteps - 1;
      auto clampt = [&](int t) { return t < last ? t : last; };
      auto rd16 = [&](int t) -> u32 { return *(const LDSP u32*)(size_t)(a2 + t * 256 + 4); };
      auto rd32 = [&](int t) -> u32 { return *(const LDSP u32*)(size_t)(a2 + t * 256); };
      auto s_write = [&](u32 a, u16 v) { *(LDSP u16*)(size_t)(simg + a) = v; };
      auto dma_e = [&](u32 eo, int t) { dma16(esrc, eo + ce, eb + (t & 3) * 1024); };
      u32 vo[4];
      auto set_vo = [&](const u32x4& so) {
#pragma unroll
        for (int j = 0; j < 4; ++j) vo[j] = so[j] + cq[j];
      };
      const f32x16 zero = {0};

      __builtin_amdgcn_s_setprio(3);
      const u32x4 so0 = lds_read16u(a1);
      const u32 eo0 = rd32(0), eo1 = rd32(clampt(1)), eo2 = rd32(clampt(2)), eo3 = rd32(clampt(3));
      u32 sa = rd16(0);
      u32 sa1 = rd16(clampt(1));
      const u32x4 so1 = lds_read16u(a1 + clampt(1) * 128);
      asm volatile("" ::"v"(pb[0][0]), "v"(pb[0][1]), "v"(pb[1][0]), "v"(pb[1][1]) : "memory");
      SB();
      set_vo(so0);
      dma_e(eo0, 0);
      dma16x4(qsrc, vo, qb);
      dma16x4(vsrc, vo, vb);
      dma_e(eo1, 1);
      dma_e(eo2, 2);
      s_write(sa, (u16)0x3F80);
      mem_fence();
      bf16x8 stf[2], sf[2], qf[4], vfr[4], ef;
#pragma unroll
      for (int s = 0; s < 2; ++s) stf[s] = read_st(s);
      LSTAMP(ws, 1, 14);
      wait_vm<6>();
      LSTAMP(ws, 1, 15);
      ef = lds_read16(eaddr);
#pragma unroll
      for (int c = 0; c < 4; ++c) qf[c] = lds_read16(qaddr[c]);
      set_vo(so1);
      arrived4(qf);
      dma16x4(qsrc, vo, qb);
      f32x16 za, zb, ea, ebv = zero, vf;
      za = mfma(ef, cw[0], zero);
      zb = mfma(ef, cw[1], zero);
      za = mfma(stf[0], pb[0][0], za);
      zb = mfma(stf[0], pb[1][0], zb);
      za = mfma(stf[1], pb[0][1], za);
      zb = mfma(stf[1], pb[1][1], zb);
      za = mfma(qf[0], sel[0], za);
      zb = mfma(qf[2], sel[0], zb);
      za = mfma(qf[1], sel[1], za);
      u32x4 m0 = {0, 0, 0, 0}, m1 = {0, 0, 0, 0};
      sf[0] = __builtin_bit_cast(bf16x8, m0);
      sf[1] = sf[0];
      dma_e(eo3, 3);
      SB();

      LSTAMP(ws, 1, 4);
      LCYC(ws, 1, 9);
      __builtin_amdgcn_s_setprio(1);
      for (int t = 0; t <= last; ++t) {
        wait_vm<5>();
#pragma unroll
        for (int c = 0; c < 4; ++c) vfr[c] = lds_read16(qaddr[c] + 4096);
        SB();
        const u32x4 so = lds_read16u(a1 + clampt(t + 2) * 128);
        const u32 eo = rd32(clampt(t + 4));
        const u32 sa2 = rd16(clampt(t + 2));
        SB();
        mfma_acc(zb, qf[3], sel[1]);
        cvt16(m0, m1, ebv);
        SB();
        mfma_acc(agg[1], sf[0], __builtin_bit_cast(bf16x8, m0));
        exp_range<0, 4>(ea, za);
        SB();
        mfma_acc(agg[1], sf[1], __builtin_bit_cast(bf16x8, m1));
        sf[0] = lds_read16(saddr[0]);
        sf[1] = lds_read16(saddr[1]);
        mem_fence();
        s_write(sa, (u16)0);
        s_write(sa1, (u16)0x3F80);
        mem_fence();
        stf[0] = read_st(0);
        stf[1] = read_st(1);
        exp_range<4, 8>(ea, za);
        SB();
        mfma_new(vf, vfr[0], sel[0]);
        ef = lds_read16(eaddr + ((t + 1) & 3) * 1024);
        exp_range<8, 12>(ea, za);
        SB();
        mfma_acc(vf, vfr[1], sel[1]);
        arrived4(vfr);
        if (t == last && pass + 1 == npass) u_offsets(vo);
        dma16x4(vsrc, vo, vb);
        wait_vm<5>();
#pragma unroll
        for (int c = 0; c < 4; ++c) qf[c] = lds_read16(qaddr[c]);
        sa = sa1;
        sa1 = sa2;
        exp_range<12, 16>(ea, za);
        SB();
        __builtin_amdgcn_s_setprio(0);
        set_vo(so);
        exp_range<0, 2>(ebv, zb);
        SB();
        mfma_acc(ea, one, one);
        exp_range<2, 5>(ebv, zb);
        SB();
        mfma_new(za, ef, cw[0]);
        exp_range<5, 8>(ebv, zb);
        SB();
        mfma_acc(za, stf[0], pb[0][0]);
        arrived4(qf);
        dma16x4(qsrc, vo, qb);
        dma_e(eo, t + 4);
        exp_range<8, 11>(ebv, zb);
        SB();
        mfma_acc(za, stf[1], pb[0][1]);
        exp_range<11, 14>(ebv, zb);
        SB();
        mfma_acc(za, qf[0], sel[0]);
        exp_range<14, 16>(ebv, zb);
        SB();
        rcp_range<0, 1>(ea);
        SB();
        mfma_acc(ebv, one, one);
        rcp_range<1, 4>(ea);
        SB();
        mfma_acc(za, qf[1], sel[1]);
        rcp_range<4, 7>(ea);
        SB();
        mfma_new(zb, ef, cw[1]);
        rcp_range<7, 10>(ea);
        SB();
        mfma_acc(zb, stf[0], pb[1][0]);
        rcp_range<10, 13>(ea);
        SB();
        mfma_acc(zb, stf[1], pb[1][1]);
        rcp_range<13, 16>(ea);
        SB();
        mul16(ea, vf);
        SB();
        __builtin_amdgcn_s_setprio(1);
        mfma_new(vf, vfr[2], sel[0]);
        cvt16(m0, m1, ea);
        SB();
        mfma_acc(vf, vfr[3], sel[1]);
        rcp_range<0, 4>(ebv);
        SB();
        mfma_acc(agg[0], sf[0], __builtin_bit_cast(bf16x8, m0));
        rcp_range<4, 8>(ebv);
        SB();
        mfma_acc(agg[0], sf[1], __builtin_bit_cast(bf16x8, m1));
        rcp_range<8, 12>(ebv);
        SB();
        mfma_acc(zb, qf[2], sel[0]);
        rcp_range<12, 16>(ebv);
        SB();
        mul16(ebv, vf);
        SB();
      }
      __builtin_amdgcn_s_setprio(0);
      cvt16(m0, m1, ebv);
      LSTAMP(ws, 1, 5);
      LCYC(ws, 1, 10);
      agg[1] = mfma(sf[0], __builtin_bit_cast(bf16x8, m0), agg[1]);
      agg[1] = mfma(sf[1], __builtin_bit_cast(bf16x8, m1), agg[1]);
      s_write(sa, (u16)0);
      wait_vm0();
    }
    if (pass + 1 < npass) wg_barrier();
  }
  STAMP(ws, 1, 6);
  LSTAMP(ws, 1, 2);

  {
    const int lane = fresh_lane(), h = lane >> 5;
    const int blk = (lane >> 4) & 1, qq = (lane & 15) >> 2, p = lane & 3;
#pragma unroll
    for (int ct = 0; ct < 2; ++ct)
#pragma unroll
      for (int s = 0; s < 2; ++s) {
        s16x4 part[2];
#pragma unroll
        for (int t = 0; t < 2; ++t) {
          const int row = 16 * s + 8 * h + 4 * t + qq;
          const int ch = 4 * ct + 2 * blk + (p >> 1);
          part[t] = lds_read_tr(vb + row * 128 + ((ch ^ ((row >> 1) & 7)) << 4) + (p & 1) * 8);
        }
        agg[ct] = mfma(sel[s], join8(part[0], part[1]), agg[ct]);
      }
  }
  {
  const int lane = fresh_lane(), h = lane >> 5, f = lane & 31;
  const u32 obase = qb + 4 * h * 128 + f * 2;
#pragma unroll
  for (int ct = 0; ct < 2; ++ct) {
    f32x2 a[6], g[6];
#pragma unroll
    for (int i = 0; i < 6; ++i) {
      a[i] = (f32x2){agg[ct][2 * i], agg[ct][2 * i + 1]};
      const f32x2 y = a[i] * (f32x2){NLOG2E, NLOG2E};
      const f32x2 d = (f32x2){__builtin_amdgcn_exp2f(y.x), __builtin_amdgcn_exp2f(y.y)} + (f32x2){1.0f, 1.0f};
      g[i] = (f32x2){__builtin_amdgcn_rcpf(d.x), __builtin_amdgcn_rcpf(d.y)};
    }
#pragma unroll
    for (int i = 0; i < 6; ++i) {
      const int r = 2 * i;
      const f32x2 o = a[i] * g[i];
      const u32 pk = pack_bf16(o.x, o.y);
      const u32 ad = obase + ((r & 3) + 8 * (r >> 2)) * 128 + ct * 64;
      *(LDSP u16*)(size_t)ad = (u16)pk;
      *(LDSP u16*)(size_t)(ad + 128) = (u16)(pk >> 16);
    }
  }
  mem_fence();
  {
    const rsrc_t outr = make_rsrc(out, (u32)N * D * 2);
#pragma unroll
    for (int i = 0; i < 3; ++i) {
      const int row = 8 * i + (lane >> 3);
      const u32x4 v = lds_read16u(qb + row * 128 + (lane & 7) * 16);
      if (row < tsz)
        __builtin_amdgcn_raw_buffer_store_b128(v, outr, (u32)(n0 + row) * (D * 2) + slice * 128 + (lane & 7) * 16, 0, OUT_AUX);
    }
  }
  }
  STAMP(ws, 1, 7);
  LSTAMP(ws, 1, 3);
  STAMP(ws, 1, 8);
}

inline void launch_k1(void* const* d_in, void* d_ws, hipStream_t stream) {
  (void)hipFuncSetAttribute((const void*)prep_kernel, hipFuncAttributeMaxDynamicSharedMemorySize, K1_LDS);
  prep_kernel<<<NPROD + NGEMM, 512, K1_LDS, stream>>>((const char*)d_in[0], (const char*)d_in[1], (const int*)d_in[2],
                                                    (const int*)d_in[3], (const char*)d_in[4], (const char*)d_in[5],
                                                    (const char*)d_in[6], (char*)d_ws);
}
inline void launch_k2(void* const* d_in, void* d_out, void* d_ws, hipStream_t stream) {
  constexpr u32 K2_DYN = K2_LDS;
  (void)hipFuncSetAttribute((const void*)edge_kernel, hipFuncAttributeMaxDynamicSharedMemorySize, K2_DYN);
  edge_kernel<<<256, 512, K2_DYN, stream>>>((const char*)d_ws, (u16*)d_out);
}

}

extern "C" void kernel_launch(void* const* d_in, const int* in_sizes, int n_in, void* d_out, int out_size,
                              void* d_ws, size_t ws_size, hipStream_t stream) {
  egc::launch_k1(d_in, d_ws, stream);
  egc::launch_k2(d_in, d_out, d_ws, stream);
}
```

```cpp
#include <hip/hip_runtime.h>
#include <hip/hip_bf16.h>
#include <stdint.h>

namespace egc {

typedef unsigned int u32;
typedef unsigned short u16;
typedef float f32x2 __attribute__((ext_vector_type(2)));
typedef float f32x16 __attribute__((ext_vector_type(16)));
typedef __bf16 bf16x2 __attribute__((ext_vector_type(2)));
typedef __bf16 bf16x4 __attribute__((ext_vector_type(4)));
typedef __bf16 bf16x8 __attribute__((ext_vector_type(8)));
typedef short s16x4 __attribute__((ext_vector_type(4)));
typedef u32 u32x2 __attribute__((ext_vector_type(2)));
typedef u32 u32x4 __attribute__((ext_vector_type(4)));
#define LDSP __attribute__((address_space(3)))

constexpr int N = 10000, D = 256, DE = 16, E = 160000;
constexpr int NPROD = 64;
constexpr int CHUNK = E / NPROD;
constexpr int NTILE = 512;
constexpr int TPG = 8;
constexpr int OFFP = 520;
constexpr int NROWP = 10016;
constexpr size_t SLICEB = (size_t)NROWP * 256;
constexpr int CAPT = 448;
constexpr float NLOG2E = -1.44269504088896340736f;

constexpr size_t WS_QV = 0;
constexpr size_t WS_PU = WS_QV + 4 * SLICEB;
constexpr size_t WS_EP = WS_PU + 4 * SLICEB;
constexpr size_t WS_REC = WS_EP + (size_t)(E + 64) * 32;
constexpr size_t WS_OFF = WS_REC + (size_t)(E + 64) * 4;
constexpr int NGROUP = NTILE / TPG;
constexpr int CAPG = 64;
constexpr size_t WS_SLOT = (WS_OFF + (size_t)NPROD * OFFP * 2 + 255) & ~(size_t)255;
constexpr size_t WS_CWF = WS_SLOT + (size_t)NGROUP * NPROD * CAPG * 4;
constexpr size_t WS_END = WS_CWF + 4 * 2 * 64 * 16;

template <int V>
struct IC {
  static constexpr int value = V;
};

__device__ __forceinline__ int tile_start(int t) { return (625 * t) >> 5; }
__device__ __forceinline__ int tile_of(int d) { return (32 * d + 31) / 625; }

__device__ __forceinline__ f32x16 mfma(bf16x8 a, bf16x8 b, f32x16 c) {
  return __builtin_amdgcn_mfma_f32_32x32x16_bf16(a, b, c, 0, 0, 0);
}
__device__ __forceinline__ u32 pack_bf16(float a, float b) {
  bf16x2 v;
  v[0] = (__bf16)a;
  v[1] = (__bf16)b;
  return __builtin_bit_cast(u32, v);
}
__device__ __forceinline__ float bf16_bits_to_f32(u16 b) { return __builtin_bit_cast(float, (u32)b << 16); }

__device__ __forceinline__ void dma16(const void* sbase, u32 voff, u32 lds) {
  asm volatile("s_mov_b32 m0, %2\n\ts_nop 0\n\tglobal_load_lds_dwordx4 %0, %1"
               :
               : "v"(voff), "s"(sbase), "s"(lds)
               : "memory");
}
typedef __amdgpu_buffer_rsrc_t rsrc_t;

#define NT_IN 1
#define XSPREAD 0
#define WARM_MODE 1
#define TBL_AUX 16
#define EP_AUX 16
#define OUT_AUX 18
__device__ __forceinline__ rsrc_t make_rsrc(const void* base, u32 bytes) {
  return __builtin_amdgcn_make_buffer_rsrc((void*)base, 0, bytes, 0x00020000);
}
__device__ __forceinline__ void store16_wt(rsrc_t r, u32 off, u32x4 v) {
  __builtin_amdgcn_raw_buffer_store_b128(v, r, off, 0, 16);
}
__device__ __forceinline__ void store4_wt(rsrc_t r, u32 off, u32 v) {
  __builtin_amdgcn_raw_buffer_store_b32(v, r, off, 0, 16);
}
__device__ __forceinline__ int fresh_lane() {
  u32 z;
  asm volatile("s_mov_b32 %0, 0" : "=s"(z));
  return (int)__builtin_amdgcn_mbcnt_hi(~0u, __builtin_amdgcn_mbcnt_lo(~0u, z));
}
__device__ __forceinline__ void bdma16x4(rsrc_t r, u32 v0, u32 v1, u32 v2, u32 v3, u32 soff, u32 lds) {
  asm volatile(
      "s_mov_b32 m0, %6\n\ts_nop 0\n\t"
      "buffer_load_dwordx4 %0, %4, %5 offen lds\n\t"
      "buffer_load_dwordx4 %1, %4, %5 offen offset:1024 lds\n\t"
      "buffer_load_dwordx4 %2, %4, %5 offen offset:2048 lds\n\t"
      "buffer_load_dwordx4 %3, %4, %5 offen offset:3072 lds"
      :
      : "v"(v0), "v"(v1), "v"(v2), "v"(v3), "s"(r), "s"(soff), "s"(lds)
      : "memory");
}
__device__ __forceinline__ void bdma16(rsrc_t r, u32 v, u32 soff, u32 lds) {
  asm volatile("s_mov_b32 m0, %3\n\ts_nop 0\n\tbuffer_load_dwordx4 %0, %1, %2 offen lds"
               :
               : "v"(v), "s"(r), "s"(soff), "s"(lds)
               : "memory");
}
__device__ __forceinline__ void mem_fence() { asm volatile("" ::: "memory"); }
__device__ __forceinline__ void wait_vm0() { asm volatile("s_waitcnt vmcnt(0)" ::: "memory"); }
__device__ __forceinline__ void wait_lgkm0() { asm volatile("s_waitcnt lgkmcnt(0)" ::: "memory"); }
__device__ __forceinline__ void wg_barrier() {
  asm volatile("s_waitcnt lgkmcnt(0)\n\ts_barrier" ::: "memory");
}
__device__ __forceinline__ bf16x8 lds_read16(u32 addr) { return *(const LDSP bf16x8*)(size_t)addr; }
__device__ __forceinline__ u32x4 lds_read16u(u32 addr) { return *(const LDSP u32x4*)(size_t)addr; }
__device__ __forceinline__ s16x4 lds_read_tr(u32 addr) {
  return __builtin_amdgcn_ds_read_tr16_b64_v4i16((LDSP s16x4*)(size_t)addr);
}
__device__ __forceinline__ bf16x8 join8(s16x4 lo, s16x4 hi) {
  u32x2 a = __builtin_bit_cast(u32x2, lo), b = __builtin_bit_cast(u32x2, hi);
  u32x4 r = {a.x, a.y, b.x, b.y};
  return __builtin_bit_cast(bf16x8, r);
}

#define STAMP(wsp, kid, i)
#define CSTAMP(wsp, kid, i, extra)
#define LSTAMP(wsp, kid, i)
#define LCYC(wsp, kid, i)

constexpr int K1_LDS = 163840;
constexpr int NGEMM = 192;

__device__ __forceinline__ u32 wave_scan(u32 v) {
  v += __builtin_amdgcn_update_dpp(0u, v, 0x111, 0xF, 0xF, false);
  v += __builtin_amdgcn_update_dpp(0u, v, 0x112, 0xF, 0xF, false);
  v += __builtin_amdgcn_update_dpp(0u, v, 0x114, 0xF, 0xF, false);
  v += __builtin_amdgcn_update_dpp(0u, v, 0x118, 0xF, 0xF, false);
  v += __builtin_amdgcn_update_dpp(0u, v, 0x142, 0xA, 0xF, false);
  v += __builtin_amdgcn_update_dpp(0u, v, 0x143, 0xC, 0xF, false);
  return v;
}

__device__ __forceinline__ void producer_block(char* smem, int pb, const int* __restrict__ src,
                                               const int* __restrict__ dst, const char* __restrict__ eattr,
                                               const char* __restrict__ gw, char* __restrict__ ws) {
  u32* hist = (u32*)smem;
  u32* wsum = hist + 512;
  u32* srec = wsum + 16;
  u16* seid = (u16*)(srec + 2560);
  u16* sgrp = seid + 2560;
  constexpr u32 EL = 32768;
  const int tid = threadIdx.x, lane = tid & 63, w = tid >> 6;
  const int e0 = pb * CHUNK;
  const rsrc_t wsr = make_rsrc(ws, (u32)WS_END);
  STAMP(ws, 0, 0);
  int dd[5], ss[5];
#pragma unroll
  for (int k = 0; k < 5; ++k) {
    const int i = tid + 512 * k;
    const int ii = i < CHUNK ? i : 0;
    dd[k] = __builtin_nontemporal_load(dst + e0 + ii);
    ss[k] = __builtin_nontemporal_load(src + e0 + ii);
  }
  hist[tid] = 0;
  wg_barrier();
  LSTAMP(ws, 0, 2);
  u32 rec[5], tl[5], rk[5];
#pragma unroll
  for (int k = 0; k < 5; ++k) {
    const int i = tid + 512 * k;
    const int t = tile_of(dd[k]);
    const u32 dl = dd[k] - tile_start(t);
    rec[k] = ((u32)ss[k] << 8) | (((dl >> 2) & 3) << 22) | (dl << 24) | ((u32)(t & 7) << 29);
    tl[k] = t;
    rk[k] = 0;
    if (i < CHUNK) rk[k] = atomicAdd(&hist[t], 1u);
  }
  {
    const char* ein = eattr + (size_t)e0 * 32;
    const u32 lds0 = (u32)(size_t)(LDSP char*)smem;
    const int wu = __builtin_amdgcn_readfirstlane(w);
#pragma unroll
    for (int k = 0; k < 10; ++k) {
      const int i = tid + 512 * k;
      asm volatile("s_mov_b32 m0, %2\n\ts_nop 0\n\tglobal_load_lds_dwordx4 %0, %1 nt"
                   :
                   : "v"((u32)(i < 2 * CHUNK ? i : 0) * 16u), "s"(ein), "s"(lds0 + EL + (wu * 64 + 512 * k) * 16)
                   : "memory");
    }
  }
  wg_barrier();
  const u32 c = hist[tid];
  const u32 inc = wave_scan(c);
  if (lane == 63) wsum[w] = inc;
  wg_barrier();
  u32 wb = 0;
#pragma unroll
  for (int j = 0; j < 8; ++j) wb += j < w ? wsum[j] : 0u;
  const u32 base = wb + inc - c;
  hist[tid] = base;
  u16* off = (u16*)(ws + WS_OFF) + pb * OFFP;
  off[tid] = (u16)base;
  if (tid < OFFP - 512) off[512 + tid] = (u16)CHUNK;
  wg_barrier();
#pragma unroll
  for (int k = 0; k < 5; ++k) {
    const int i = tid + 512 * k;
    if (i < CHUNK) {
      const u32 p = hist[tl[k]] + rk[k];
      srec[p] = rec[k];
      seid[p] = (u16)i;
      sgrp[p] = (u16)(tl[k] >> 3);
    }
  }
  LSTAMP(ws, 0, 3);
  wait_vm0();
  wg_barrier();
  LSTAMP(ws, 0, 4);
#pragma unroll
  for (int k = 0; k < 10; ++k) {
    const int i = tid + 512 * k;
    if (i < 2 * CHUNK)
      __builtin_amdgcn_raw_buffer_store_b128(*(const u32x4*)(smem + EL + (int)seid[i >> 1] * 32 + (i & 1) * 16), wsr,
                                             (u32)WS_EP + (u32)e0 * 32u + i * 16, 0, EP_AUX);
  }
#pragma unroll
  for (int k = 0; k < 5; ++k) {
    const int p = tid + 512 * k;
    if (p < CHUNK) {
      const u32 g = sgrp[p], rel = p - hist[g * 8];
      if (rel < (u32)CAPG) store4_wt(wsr, (u32)WS_SLOT + ((g * NPROD + pb) * CAPG + rel) * 4u, srec[p]);
    }
  }
  if (tid < CHUNK / 4) store16_wt(wsr, (u32)WS_REC + (u32)e0 * 4u + tid * 16, *(const u32x4*)(srec + tid * 4));
  if (tid + 512 < CHUNK / 4)
    store16_wt(wsr, (u32)WS_REC + (u32)e0 * 4u + (tid + 512) * 16, *(const u32x4*)(srec + (tid + 512) * 4));
  STAMP(ws, 0, 3);
  if (pb == 1) {
    const int sl = tid >> 7, ct = (tid >> 6) & 1, hh = lane >> 5, ff = lane & 31;
    u32x4 v;
#pragma unroll
    for (int j = 0; j < 4; ++j) {
      const u16* wrow = (const u16*)gw + (512 + 8 * hh + 2 * j) * D + sl * 64 + ct * 32 + ff;
      v[j] = pack_bf16(bf16_bits_to_f32(wrow[0]) * NLOG2E, bf16_bits_to_f32(wrow[D]) * NLOG2E);
    }
    store16_wt(wsr, (u32)WS_CWF + tid * 16, v);
  }
  if (pb == 0) {
    const u32x4 z = {0, 0, 0, 0};
    if (tid < 64) *(u32x4*)(ws + WS_QV + (tid >> 4) * SLICEB + (size_t)N * 256 + (tid & 15) * 16) = z;
    if (tid >= 64 && tid < 66) *(u32x4*)(ws + WS_EP + (size_t)E * 32 + (tid - 64) * 16) = z;
  }
}

__device__ __forceinline__ void gemm_block(char* smem, int gb, const char* __restrict__ x,
                                           const char* __restrict__ uw, const char* __restrict__ vw,
                                           const char* __restrict__ gw, char* __restrict__ ws) {
  const int tid = threadIdx.x, lane = tid & 63;
  const int w = __builtin_amdgcn_readfirstlane(tid >> 6);
  const u32 lds0 = (u32)(size_t)(LDSP char*)smem;
  const int q = gb >> 3, xcd = gb & 7;
  const int cg = q / 3;
  const int type = cg & 3, half = cg >> 2;
  const int rp0 = (q - 3 * cg) * 8 + xcd;
  const int cw4 = w & 3, rg = w >> 2;
  const char* W = (type == 0 ? gw + 256 * 512 : type == 1 ? vw : type == 2 ? gw : uw) + half * 256;
  const float scale = (type & 1) ? 1.0f : NLOG2E;
  const int ctg = half * 4 + cw4;
  const rsrc_t xr = make_rsrc(x, (u32)N * 512u);
  const rsrc_t dr = make_rsrc(ws + (type < 2 ? WS_QV : WS_PU) + (type & 1) * 128 + (size_t)(ctg >> 1) * SLICEB,
                              (u32)N * 256u - 128u);

  constexpr u32 TB = 131072;
  const u32 xlane = (u32)((lane >> 5) * 512) + ((u32)(((lane & 31) ^ (lane >> 5)) << 4) ^ (u32)((cw4 & 1) << 7));
  auto x_soff = [&](int job) { return (u32)((rp0 + 24 * job) * 64 + rg * 32) * 512u + (u32)cw4 * 4096u; };
  auto x_lds = [&](int job) { return lds0 + ((job + 2) & 3) * 32768 + rg * 16384 + cw4 * 4096; };
  auto load_x = [&](int job) { bdma16x4(xr, xlane, xlane ^ 32u, xlane ^ 64u, xlane ^ 96u, x_soff(job), x_lds(job)); };
  STAMP(ws, 0, 0);
  auto load_w = [&](int q) {
#pragma unroll
    for (int i = 0; i < 2; ++i) {
      const int j = 16 * q + 2 * w + i;
      const int k = 4 * j + (lane >> 4);
      const u32 voff = (u32)k * 512u + (u32)(((lane & 15) ^ ((k & 3) << 2)) * 16);
      dma16(W, voff, lds0 + j * 1024);
    }
  };
  load_w(0);
  load_x(0);
  load_w(1);
  load_w(2);
  load_w(3);
  load_x(1);

  const int h = lane >> 5, blk = (lane >> 4) & 1, qq = (lane & 15) >> 2, p = lane & 3;
  const int n = lane & 31;
  u32 xaddr[8];
#pragma unroll
  for (int c = 0; c < 8; ++c) xaddr[c] = lds0 + rg * 16384 + n * 512 + (((2 * c + h) ^ (n & 15)) << 4);
  auto xfrag = [&](int job, int ks) { return lds_read16(xaddr[ks & 7] + ((job + 2) & 3) * 32768 + (ks >> 3) * 256); };
  const u32 tb = lds0 + TB + (w >> 1) * 8192;
  const int cpar = w & 1;
  const u32 tput = tb + n * 128, tsw = (4 * cpar + 2 * h) ^ (n & 7);
  const u32 tget = tb + (16 * cpar + (lane >> 3)) * 128 + (((lane & 7) ^ (lane >> 3)) << 4);
  const u32 slane = (u32)((lane >> 3) * 256 + (lane & 7) * 16);
  f32x16 accs[2];
  bf16x8 xf[8], wf[16];

  {
    const int col0 = cw4 * 32 + 16 * (p & 1) + 8 * blk + 4 * (p >> 1);
    const u32 a0 = lds0 + (8 * h + qq) * 256 + ((((col0 >> 3) ^ (qq << 2)) << 4) | ((col0 & 7) << 1));
    auto quarter = [&](auto qc, auto nyoung) {
      constexpr int q = decltype(qc)::value;
      asm volatile("s_waitcnt vmcnt(%0)" ::"n"(decltype(nyoung)::value) : "memory");
      wg_barrier();
      if (q == 2) load_x(2);
#pragma unroll
      for (int ks = 4 * q; ks < 4 * q + 4; ++ks) {
        wf[ks] = join8(lds_read_tr(a0 + ks * 4096), lds_read_tr(a0 + ks * 4096 + 1024));
        xf[ks & 7] = xfrag(0, ks);
      }
      if (q == 3) {
#pragma unroll
        for (int ks = 0; ks < 4; ++ks) xf[ks] = xfrag(1, ks);
      }
      __builtin_amdgcn_sched_barrier(0);
#pragma unroll
      for (int ks = 4 * q; ks < 4 * q + 4; ++ks) {
        if (ks == 0) {
          const f32x16 zero = {0};
          accs[0] = mfma(wf[0], xf[0], zero);
        } else {
          accs[0] = mfma(wf[ks], xf[ks & 7], accs[0]);
        }
      }
      asm volatile("" : "+v"(accs[0]));
      __builtin_amdgcn_sched_barrier(0);
    };
    quarter(IC<0>{}, IC<10>{});
    LSTAMP(ws, 0, 2);
    quarter(IC<1>{}, IC<8>{});
    quarter(IC<2>{}, IC<6>{});
    quarter(IC<3>{}, IC<4>{});
    LSTAMP(ws, 0, 4);
    LCYC(ws, 0, 8);
  }

  auto job_body = [&](auto jc) {
    constexpr int job = decltype(jc)::value;
    f32x16& acc = accs[job & 1];
    const f32x16& accp = accs[(job + 1) & 1];
    u32x4 c0, c1, o[2];
    constexpr int young = job == 1 ? 0 : job == 2 ? 4 : job == 3 ? 6 : job == 4 ? 8 : 4;
    if (job <= 5) asm volatile("s_waitcnt vmcnt(%0)" ::"n"(young) : "memory");
    wg_barrier();
    const u32 xs = x_soff(job + 3), xl = x_lds(job + 3);
    const u32 ds = (u32)((rp0 + 24 * (job - 2)) * 64 + rg * 32 + 16 * cpar) * 256u;
    auto gap = [&](auto kc) {
      constexpr int ks = decltype(kc)::value;
      if (job < 7) {
        if (ks == 0) {
          const f32x16 zero = {0};
          acc = mfma(wf[0], xf[0], zero);
        } else {
          acc = mfma(wf[ks], xf[ks & 7], acc);
        }
        asm volatile("" : "+v"(acc));
        __builtin_amdgcn_sched_barrier(0);
        if (ks < 12) xf[(ks + 4) & 7] = xfrag(job, ks + 4);
        else if (job < 6) xf[(ks + 4) & 7] = xfrag(job + 1, ks - 12);
      }
      if (job >= 1 && job <= 7) {
        if (ks >= 1 && ks <= 4) {
          constexpr int i = ks - 1;
          c0[i] = pack_bf16(accp[2 * i] * scale, accp[2 * i + 1] * scale);
          c1[i] = pack_bf16(accp[8 + 2 * i] * scale, accp[8 + 2 * i + 1] * scale);
        }
        if (ks == 5) {
          const u32 t = tput + ((job - 1) & 1) * 4096;
          *(LDSP u32x4*)(size_t)(t + (tsw << 4)) = c0;
          *(LDSP u32x4*)(size_t)(t + ((tsw ^ 1) << 4)) = c1;
        }
      }
      if (job >= 2 && ks == 0) {
#pragma unroll
        for (int i = 0; i < 2; ++i) o[i] = lds_read16u(tget + ((job - 2) & 1) * 4096 + i * 1024);
      }
      if (job == 1 && ks < 4)
        bdma16(xr, (xlane ^ (u32)(ks << 5)) + ks * 1024, x_soff(3), x_lds(3) + ks * 1024);
      constexpr int k0 = job == 1 ? 4 : 0;
      if (job <= 3 && ks >= k0 && ks <= k0 + 3)
        bdma16(xr, (xlane ^ (u32)((ks - k0) << 5)) + (ks - k0) * 1024, xs, xl + (ks - k0) * 1024);
      constexpr int si = ks == 10 ? 0 : ks == 11 ? 1 : -1;
      if (job >= 2 && si >= 0) {
        __builtin_amdgcn_raw_buffer_store_b128(o[si & 1], dr, slane + (si & 1) * 2048, ds, TBL_AUX);
      }
      __builtin_amdgcn_sched_barrier(0);
    };
    gap(IC<0>{}); gap(IC<1>{}); gap(IC<2>{}); gap(IC<3>{}); gap(IC<4>{}); gap(IC<5>{}); gap(IC<6>{}); gap(IC<7>{});
    gap(IC<8>{}); gap(IC<9>{}); gap(IC<10>{}); gap(IC<11>{}); gap(IC<12>{}); gap(IC<13>{}); gap(IC<14>{});
    gap(IC<15>{});
    if (job == 3) LSTAMP(ws, 0, 5);
    if (job == 6) { LSTAMP(ws, 0, 6); LCYC(ws, 0, 9); }
  };
  job_body(IC<1>{}); job_body(IC<2>{}); job_body(IC<3>{}); job_body(IC<4>{}); job_body(IC<5>{}); job_body(IC<6>{});
  {
    const u32 d6 = lds0 + 98304 + (w >> 1) * 4096 - tb;
    const f32x16& al = accs[0];
    u32x4 c0, c1, o[4];
#pragma unroll
    for (int i = 0; i < 4; ++i) {
      c0[i] = pack_bf16(al[2 * i] * scale, al[2 * i + 1] * scale);
      c1[i] = pack_bf16(al[8 + 2 * i] * scale, al[8 + 2 * i + 1] * scale);
    }
    *(LDSP u32x4*)(size_t)(tput + d6 + (tsw << 4)) = c0;
    *(LDSP u32x4*)(size_t)(tput + d6 + ((tsw ^ 1) << 4)) = c1;
    wg_barrier();
#pragma unroll
    for (int i = 0; i < 2; ++i) {
      o[i] = lds_read16u(tget + 4096 + i * 1024);
      o[2 + i] = lds_read16u(tget + d6 + i * 1024);
    }
    const u32 ds5 = (u32)((rp0 + 24 * 5) * 64 + rg * 32 + 16 * cpar) * 256u;
    const u32 ds6 = (u32)((rp0 + 24 * 6) * 64 + rg * 32 + 16 * cpar) * 256u;
#pragma unroll
    for (int i = 0; i < 2; ++i) {
      __builtin_amdgcn_raw_buffer_store_b128(o[i], dr, slane + i * 2048, ds5, TBL_AUX);
      __builtin_amdgcn_raw_buffer_store_b128(o[2 + i], dr, slane + i * 2048, ds6, TBL_AUX);
    }
  }
}

__global__ void __launch_bounds__(512, 2)
prep_kernel(const char* __restrict__ x, const char* __restrict__ eattr, const int* __restrict__ src,
            const int* __restrict__ dst, const char* __restrict__ uw, const char* __restrict__ vw,
            const char* __restrict__ gw, char* __restrict__ ws) {
  extern __shared__ __attribute__((aligned(16))) char smem[];
  const int b = blockIdx.x;
  LSTAMP(ws, 0, 0);
  if (b < NPROD)
    producer_block(smem, b, src, dst, eattr, gw, ws);
  else
    gemm_block(smem, b - NPROD, x, uw, vw, gw, ws);
  LSTAMP(ws, 0, 1);
}

constexpr u32 L_QV = 0;
constexpr u32 L_E = 65536;
constexpr u32 L_S = L_E + 8 * 4096;
constexpr u32 L_A1 = L_S + 8 * 2048 + 128;
constexpr int LDUMMY = TPG * CAPT;
constexpr u32 L_A2 = L_A1 + (LDUMMY + 16) * 4;
constexpr u32 L_DL = L_A2 + (LDUMMY + 16) * 8;
constexpr u32 L_GS = L_DL + 8 * 64 * 8;
constexpr u32 L_GE = L_GS + 256;
constexpr u32 L_TT = L_GE + 256;
constexpr u32 L_PROG = L_TT + 64;
constexpr u32 L_JUNK = L_PROG + 64;
constexpr u32 K2_LDS = L_JUNK + 256;
static_assert(K2_LDS <= 163840, "LDS budget");

#define SB() __builtin_amdgcn_sched_barrier(0)
__device__ __forceinline__ void mfma_acc(f32x16& c, bf16x8 a, bf16x8 b) {
  c = mfma(a, b, c);
  asm volatile("" : "+v"(c));
  SB();
}
__device__ __forceinline__ void mfma_new(f32x16& c, bf16x8 a, bf16x8 b) {
  const f32x16 zero = {0};
  c = mfma(a, b, zero);
  asm volatile("" : "+v"(c));
  SB();
}
__device__ __forceinline__ void arrived4(const bf16x8 (&v)[4]) {
  asm volatile("" ::"v"(v[0]), "v"(v[1]), "v"(v[2]), "v"(v[3]) : "memory");
}
__device__ __forceinline__ void dma16x4(const void* sbase, const u32 (&vo)[4], u32 lds) {
  asm volatile(
      "s_mov_b32 m0, %5\n\ts_nop 0\n\t"
      "global_load_lds_dwordx4 %0, %4\n\t"
      "global_load_lds_dwordx4 %1, %4 offset:1024\n\t"
      "global_load_lds_dwordx4 %2, %4 offset:2048\n\t"
      "global_load_lds_dwordx4 %3, %4 offset:3072"
      :
      : "v"(vo[0]), "v"(vo[1]), "v"(vo[2]), "v"(vo[3]), "s"(sbase), "s"(lds)
      : "memory");
}
template <int A, int B>
__device__ __forceinline__ void exp_range(f32x16& d, const f32x16& s) {
#pragma unroll
  for (int i = A; i < B; ++i) d[i] = __builtin_amdgcn_exp2f(s[i]);
}
template <int A, int B>
__device__ __forceinline__ void rcp_range(f32x16& d) {
#pragma unroll
  for (int i = A; i < B; ++i) d[i] = __builtin_amdgcn_rcpf(d[i]);
}
__device__ __forceinline__ void mul16(f32x16& g, const f32x16& v) {
#pragma unroll
  for (int i = 0; i < 8; ++i) {
    const f32x2 a = (f32x2){g[2 * i], g[2 * i + 1]} * (f32x2){v[2 * i], v[2 * i + 1]};
    g[2 * i] = a.x;
    g[2 * i + 1] = a.y;
  }
  asm volatile("" : "+v"(g));
}
__device__ __forceinline__ void cvt16(u32x4& m0, u32x4& m1, const f32x16& d) {
#pragma unroll
  for (int i = 0; i < 4; ++i) {
    m0[i] = pack_bf16(d[2 * i], d[2 * i + 1]);
    m1[i] = pack_bf16(d[8 + 2 * i], d[8 + 2 * i + 1]);
  }
  asm volatile("" : "+v"(m0), "+v"(m1));
}
template <int NV>
__device__ __forceinline__ void wait_vm() { asm volatile("s_waitcnt vmcnt(%0)" ::"n"(NV) : "memory"); }
template <int NV>
__device__ __forceinline__ void wait_lgkm() { asm volatile("s_waitcnt lgkmcnt(%0)" ::"n"(NV) : "memory"); }

__global__ void __launch_bounds__(512, 2)
edge_kernel(const char* __restrict__ ws, u16* __restrict__ out) {
  extern __shared__ __attribute__((aligned(16))) char smem[];
  const int tid = threadIdx.x, lane = tid & 63;
  const int w = __builtin_amdgcn_readfirstlane(tid >> 6);
  const u32 lds0 = (u32)(size_t)(LDSP char*)smem;
  const int b = blockIdx.x, xcd = b & 7;
  const int slice = xcd & 3, group = (b >> 3) * 2 + (xcd >> 2);
  const int h = lane >> 5, f = lane & 31;
  STAMP(ws, 1, 0);
  LSTAMP(ws, 1, 0);

  const int pbk = tid >> 3, pj = tid & 7;
  u32 rec0[8], o0, o1;
  u32x4 cwr[2];
  {
    const char* sbase = ws + WS_SLOT + (size_t)group * NPROD * CAPG * 4;
    const u32 so = (u32)(pbk * CAPG + pj) * 4u;
    asm volatile(
        "global_load_dword %0, %8, %9\n\tglobal_load_dword %1, %8, %9 offset:32\n\t"
        "global_load_dword %2, %8, %9 offset:64\n\tglobal_load_dword %3, %8, %9 offset:96\n\t"
        "global_load_dword %4, %8, %9 offset:128\n\tglobal_load_dword %5, %8, %9 offset:160\n\t"
        "global_load_dword %6, %8, %9 offset:192\n\tglobal_load_dword %7, %8, %9 offset:224"
        : "=&v"(rec0[0]), "=&v"(rec0[1]), "=&v"(rec0[2]), "=&v"(rec0[3]), "=&v"(rec0[4]), "=&v"(rec0[5]),
          "=&v"(rec0[6]), "=&v"(rec0[7])
        : "v"(so), "s"(sbase)
        : "memory");
    const char* obase = ws + WS_OFF + (group * TPG + w) * 2;
    const u32 oo = (u32)lane * (OFFP * 2);
    asm volatile("global_load_ushort %0, %2, %3\n\tglobal_load_ushort %1, %2, %3 offset:2"
                 : "=&v"(o0), "=&v"(o1)
                 : "v"(oo), "s"(obase)
                 : "memory");
    const char* cbase = ws + WS_CWF + slice * 2048;
    const u32 co = (u32)lane * 16u;
    asm volatile("global_load_dwordx4 %0, %2, %3\n\tglobal_load_dwordx4 %1, %2, %3 offset:1024"
                 : "=&v"(cwr[0]), "=&v"(cwr[1])
                 : "v"(co), "s"(cbase)
                 : "memory");
  }
  {
    const u32x4 z = {0, 0, 0, 0};
    *(LDSP u32x4*)(size_t)(lds0 + L_S + w * 2048 + lane * 16) = z;
    *(LDSP u32x4*)(size_t)(lds0 + L_S + w * 2048 + 1024 + lane * 16) = z;
  }
  bf16x8 cw[2], sel[2], one;
#pragma unroll
  for (int s = 0; s < 2; ++s)
#pragma unroll
    for (int j = 0; j < 8; ++j) sel[s][j] = (__bf16)(f == 16 * s + 8 * h + j ? 1.0f : 0.0f);
#pragma unroll
  for (int j = 0; j < 8; ++j) one[j] = (__bf16)((j == 0 && h == 0) ? 1.0f : 0.0f);
  u32 cq[4];
#pragma unroll
  for (int j = 0; j < 4; ++j) cq[j] = (u32)(((lane & 7) ^ (4 * (j & 1) + (lane >> 4))) * 16 + 3072 - 1024 * j);
  u32 ce = (u32)(((lane & 1) ^ ((lane >> 4) & 1)) * 16);
  u32 qaddr[4], saddr[2], staddr[2], eaddr, a1, a2, paddr;
  {
    const int sr = (f & 0x13) | ((f & 4) << 1) | ((f & 8) >> 1);
    const int sq = ((sr & 7) << 2) | (sr >> 3);
    const int fsw = (sr >> 1) & 7;
    eaddr = lds0 + L_E + sq * 32 + ((h ^ ((sq >> 3) & 1)) << 4);
#pragma unroll
    for (int c = 0; c < 4; ++c) qaddr[c] = lds0 + L_QV + sr * 128 + (((2 * c + h) ^ fsw) << 4);
    const int blk = (lane >> 4) & 1, qq = (lane & 15) >> 2, p = lane & 3;
    const int c0 = 16 * blk + 8 * (p & 1) + 4 * (p >> 1);
#pragma unroll
    for (int s = 0; s < 2; ++s) {
      saddr[s] = lds0 + L_S + f * 64 + (((2 * s + h) ^ ((f >> 2) & 3)) << 4);
      const int nu = 8 * s + 4 * h + qq;
      staddr[s] = lds0 + L_S + nu * 64 + (((c0 >> 3) ^ ((nu >> 2) & 3)) << 4) + ((c0 & 7) << 1);
    }
    a1 = lds0 + L_A1 + (lane >> 3) * 16;
    a2 = lds0 + L_A2 + (lane >> 1) * 8;
    paddr = lds0 + L_QV + (4 * h + qq) * 128 + (16 * blk + 4 * p) * 2;
  }
  asm volatile("" : "+v"(sel[0]), "+v"(sel[1]), "+v"(one), "+v"(cq[0]), "+v"(cq[1]), "+v"(cq[2]), "+v"(cq[3]), "+v"(ce));
  asm volatile("" : "+v"(qaddr[0]), "+v"(qaddr[1]), "+v"(qaddr[2]), "+v"(qaddr[3]), "+v"(saddr[0]), "+v"(saddr[1]),
               "+v"(staddr[0]), "+v"(staddr[1]), "+v"(eaddr), "+v"(a1), "+v"(a2), "+v"(paddr));
  SB();
  asm volatile("s_waitcnt vmcnt(0)"
               : "+v"(rec0[0]), "+v"(rec0[1]), "+v"(rec0[2]), "+v"(rec0[3]), "+v"(rec0[4]), "+v"(rec0[5]),
                 "+v"(rec0[6]), "+v"(rec0[7]), "+v"(o0), "+v"(o1), "+v"(cwr[0]), "+v"(cwr[1])
               :
               : "memory");
  SB();
  LSTAMP(ws, 1, 11);
  {
    const int tile = group * TPG + w;
    const int n0w = tile_start(tile), tszw = tile_start(tile + 1) - n0w;
    const char* pu = ws + WS_PU + (size_t)slice * SLICEB;
    const u32 rb = lds0 + L_QV + w * 8192;
#pragma unroll
    for (int j = 0; j < 4; ++j) {
      const int row = 8 * j + (lane >> 3);
      const int node = n0w + (row < tszw ? row : tszw - 1);
      const u32 voff = (u32)node * 256u + (lane & 7) * 16;
      dma16(pu, voff, rb + j * 1024);
    }
    constexpr int ROWS = NROWP / 32;
    const int idx = w * 64 + lane;
    const u32 row = (u32)(b >> 3) * ROWS + (u32)(idx < ROWS ? idx : ROWS - 1);
    asm volatile("s_mov_b32 m0, %2\n\ts_nop 0\n\tglobal_load_lds_dword %0, %1"
                 :
                 : "v"(row * 256u), "s"(ws + WS_QV + (size_t)slice * SLICEB), "s"(lds0 + L_JUNK)
                 : "memory");
  }
  cw[0] = __builtin_bit_cast(bf16x8, cwr[0]);
  cw[1] = __builtin_bit_cast(bf16x8, cwr[1]);
  const int cnt = (int)o1 - (int)o0;
  const int inc = (int)wave_scan((u32)cnt);
  u32x2* dl = (u32x2*)(smem + L_DL);
  {
    const int delta = inc - cnt - (int)o0;
    dl[w * 64 + lane] = (u32x2){(u32)(delta + w * CAPT), (u32)min((int)o1, CAPT - delta)};
  }
  if (w == 0) ((int*)(smem + L_GS))[lane] = (int)o0;
  if (w == 7) ((int*)(smem + L_GE))[lane] = (int)o1;
  if (lane == 63) ((int*)(smem + L_TT))[w] = inc;
  STAMP(ws, 1, 1);
  wg_barrier();
  STAMP(ws, 1, 2);
  LSTAMP(ws, 1, 6);

  const int* tt = (const int*)(smem + L_TT);
  int tau, mytot, maxt;
  {
    const int ti = tt[lane >> 3], tj = tt[lane & 7];
    const bool before = ti > tj || (ti == tj && (lane >> 3) < (lane & 7));
    const unsigned long long m = __ballot(before);
    const int rank = __popcll(m & (0x0101010101010101ull << (lane & 7)));
    const int want = w < 4 ? w : 11 - w;
    tau = __builtin_ctzll(__ballot(lane < 8 && rank == want));
    mytot = __builtin_amdgcn_readlane(tj, tau);
    maxt = __builtin_amdgcn_readlane(tj, __builtin_ctzll(__ballot(lane < 8 && rank == 0)));
  }
  const int npass = maxt > CAPT ? (maxt + CAPT - 1) / CAPT : 1;
  const int tile = group * TPG + tau;
  const int n0 = tile_start(tile), tsz = tile_start(tile + 1) - n0;
  const u32 qb = lds0 + L_QV + tau * 8192, vb = qb + 4096;
  const u32* grec = (const u32*)(ws + WS_REC);

  const char* qsrc = ws + WS_QV + (size_t)slice * SLICEB - 3072;
  const char* vsrc = qsrc + 128;
  const char* esrc = ws + WS_EP;
  const u32 eb = lds0 + L_E + tau * 4096;
  const u32 simg = lds0 + L_S;
  eaddr += tau * 4096;
#pragma unroll
  for (int c = 0; c < 4; ++c) qaddr[c] += tau * 8192;
#pragma unroll
  for (int s = 0; s < 2; ++s) {
    saddr[s] += tau * 2048;
    staddr[s] += tau * 2048;
  }
  a1 += tau * (CAPT * 4);
  a2 += tau * (CAPT * 8);
  paddr += tau * 8192;
  auto read_st = [&](int s) { return join8(lds_read_tr(staddr[0] + s * 1024), lds_read_tr(staddr[1] + s * 1024)); };
  STAMP(ws, 1, 3);
  LSTAMP(ws, 1, 7);

  auto build_lists = [&](int pass, bool first) {
    u32* A1 = (u32*)(smem + L_A1);
    u32x2* A2 = (u32x2*)(smem + L_A2);
    const int tb = threadIdx.x >> 3, tj = threadIdx.x & 7;
    const int bs0 = ((const int*)(smem + L_GS))[tb], be0 = ((const int*)(smem + L_GE))[tb];
    const u32x2* dlb = dl + tb;
    const int pos0 = tb * CHUNK;
    auto put = [&](u32 rec, int i, u32 li, bool ok) {
      const u32 idx = ok ? li : (u32)(LDUMMY + (threadIdx.x & 15));
      A1[idx] = rec & 0x3FFF00u;
      const u32 sa = ((rec >> 18) & 0x3FF0u) ^ (((li & 3) << 4) | ((li >> 1) & 0xE));
      A2[idx] = (u32x2){(u32)(pos0 + i) << 5, sa};
    };
    int i0 = bs0 + tj;
    if (first) {
      u32x2 d[8];
#pragma unroll
      for (int k = 0; k < 6; ++k) d[k] = dlb[(rec0[k] >> 29) * 64];
#pragma unroll
      for (int k = 0; k < 6; ++k) {
        const int i = i0 + 8 * k;
        put(rec0[k], i, (u32)i + d[k].x, i < (int)d[k].y);
      }
#pragma unroll
      for (int k = 6; k < 8; ++k) {
        const int i = i0 + 8 * k;
        if (__any(i < be0)) {
          const u32x2 dk = dlb[(rec0[k] >> 29) * 64];
          put(rec0[k], i, (u32)i + dk.x, i < (int)dk.y);
        }
      }
      i0 += CAPG;
    }
    for (int i = i0; i < be0; i += 8) {
      const u32 rec = grec[pos0 + i];
      const u32 t = rec >> 29;
      const u32 rel = (u32)i + dlb[t * 64].x - t * CAPT - (u32)(pass * CAPT);
      put(rec, i, t * CAPT + rel, rel < (u32)CAPT);
    }
    int tw = ((const int*)(smem + L_TT))[w] - pass * CAPT;
    tw = tw < 0 ? 0 : tw;
    const int ln = threadIdx.x & 63;
    if (tw < CAPT && ln < 32) {
      const int li = (tw & ~31) + ln;
      if (li >= tw) {
        A1[w * CAPT + li] = (u32)N * 256u;
        A2[w * CAPT + li] = (u32x2){(u32)E * 32u, 8u * 2048u};
      }
    }
  };
  build_lists(0, true);
  LSTAMP(ws, 1, 12);
  wait_vm<1>();
  LSTAMP(ws, 1, 13);
  STAMP(ws, 1, 4);
  wg_barrier();
  bf16x8 pb[2][2];
#pragma unroll
  for (int ct = 0; ct < 2; ++ct)
#pragma unroll
    for (int s = 0; s < 2; ++s) {
      const u32 a = paddr + s * 2048 + ct * 64;
      pb[ct][s] = join8(lds_read_tr(a), lds_read_tr(a + 8 * 128));
    }
  f32x16 agg[2] = {{0}, {0}};
  auto u_offsets = [&](u32 (&o)[4]) {
    const int ln = fresh_lane();
#pragma unroll
    for (int j = 0; j < 4; ++j) {
      const int row = 8 * j + (ln >> 3);
      o[j] = (u32)(WS_PU - WS_QV) + (u32)(n0 + (row < tsz ? row : tsz - 1)) * 256u + cq[j];
    }
  };

  for (int pass = 0; pass < npass; ++pass) {
    if (pass > 0) {
      build_lists(pass, false);
      wg_barrier();
    }
    STAMP(ws, 1, 5);
    LSTAMP(ws, 1, 1);

    int rem = mytot - pass * CAPT;
    rem = rem < 0 ? 0 : (rem > CAPT ? CAPT : rem);
    const int nsteps = __builtin_amdgcn_readfirstlane(rem > 0 ? (rem + 31) >> 5 : 1);
    {
      const int last = nsteps - 1;
      auto clampt = [&](int t) { return t < last ? t : last; };
      auto rd16 = [&](int t) -> u32 { return *(const LDSP u32*)(size_t)(a2 + t * 256 + 4); };
      auto rd32 = [&](int t) -> u32 { return *(const LDSP u32*)(size_t)(a2 + t * 256); };
      auto s_write = [&](u32 a, u16 v) { *(LDSP u16*)(size_t)(simg + a) = v; };
      auto dma_e = [&](u32 eo, int t) { dma16(esrc, eo + ce, eb + (t & 3) * 1024); };
      u32 vo[4];
      auto set_vo = [&](const u32x4& so) {
#pragma unroll
        for (int j = 0; j < 4; ++j) vo[j] = so[j] + cq[j];
      };
      const f32x16 zero = {0};

      __builtin_amdgcn_s_setprio(3);
      const u32x4 so0 = lds_read16u(a1);
      const u32 eo0 = rd32(0), eo1 = rd32(clampt(1)), eo2 = rd32(clampt(2)), eo3 = rd32(clampt(3));
      u32 sa = rd16(0);
      u32 sa1 = rd16(clampt(1));
      const u32x4 so1 = lds_read16u(a1 + clampt(1) * 128);
      asm volatile("" ::"v"(pb[0][0]), "v"(pb[0][1]), "v"(pb[1][0]), "v"(pb[1][1]) : "memory");
      SB();
      set_vo(so0);
      dma_e(eo0, 0);
      dma16x4(qsrc, vo, qb);
      dma16x4(vsrc, vo, vb);
      dma_e(eo1, 1);
      dma_e(eo2, 2);
      s_write(sa, (u16)0x3F80);
      mem_fence();
      bf16x8 stf[2], sf[2], qf[4], vfr[4], ef;
#pragma unroll
      for (int s = 0; s < 2; ++s) stf[s] = read_st(s);
      LSTAMP(ws, 1, 14);
      wait_vm<6>();
      LSTAMP(ws, 1, 15);
      ef = lds_read16(eaddr);
#pragma unroll
      for (int c = 0; c < 4; ++c) qf[c] = lds_read16(qaddr[c]);
      set_vo(so1);
      arrived4(qf);
      dma16x4(qsrc, vo, qb);
      f32x16 za, zb, ea, ebv = zero, vf;
      za = mfma(ef, cw[0], zero);
      zb = mfma(ef, cw[1], zero);
      za = mfma(stf[0], pb[0][0], za);
      zb = mfma(stf[0], pb[1][0], zb);
      za = mfma(stf[1], pb[0][1], za);
      zb = mfma(stf[1], pb[1][1], zb);
      za = mfma(qf[0], sel[0], za);
      zb = mfma(qf[2], sel[0], zb);
      za = mfma(qf[1], sel[1], za);
      u32x4 m0 = {0, 0, 0, 0}, m1 = {0, 0, 0, 0};
      sf[0] = __builtin_bit_cast(bf16x8, m0);
      sf[1] = sf[0];
      dma_e(eo3, 3);
      SB();

      LSTAMP(ws, 1, 4);
      LCYC(ws, 1, 9);
      __builtin_amdgcn_s_setprio(1);
      for (int t = 0; t <= last; ++t) {
        wait_vm<5>();
#pragma unroll
        for (int c = 0; c < 4; ++c) vfr[c] = lds_read16(qaddr[c] + 4096);
        SB();
        const u32x4 so = lds_read16u(a1 + clampt(t + 2) * 128);
        const u32 eo = rd32(clampt(t + 4));
        const u32 sa2 = rd16(clampt(t + 2));
        SB();
        mfma_acc(zb, qf[3], sel[1]);
        cvt16(m0, m1, ebv);
        SB();
        mfma_acc(agg[1], sf[0], __builtin_bit_cast(bf16x8, m0));
        exp_range<0, 4>(ea, za);
        SB();
        mfma_acc(agg[1], sf[1], __builtin_bit_cast(bf16x8, m1));
        sf[0] = lds_read16(saddr[0]);
        sf[1] = lds_read16(saddr[1]);
        mem_fence();
        s_write(sa, (u16)0);
        s_write(sa1, (u16)0x3F80);
        mem_fence();
        stf[0] = read_st(0);
        stf[1] = read_st(1);
        exp_range<4, 8>(ea, za);
        SB();
        mfma_new(vf, vfr[0], sel[0]);
        ef = lds_read16(eaddr + ((t + 1) & 3) * 1024);
        exp_range<8, 12>(ea, za);
        SB();
        mfma_acc(vf, vfr[1], sel[1]);
        arrived4(vfr);
        if (t == last && pass + 1 == npass) u_offsets(vo);
        dma16x4(vsrc, vo, vb);
        wait_vm<5>();
#pragma unroll
        for (int c = 0; c < 4; ++c) qf[c] = lds_read16(qaddr[c]);
        sa = sa1;
        sa1 = sa2;
        exp_range<12, 16>(ea, za);
        SB();
        __builtin_amdgcn_s_setprio(0);
        set_vo(so);
        exp_range<0, 2>(ebv, zb);
        SB();
        mfma_acc(ea, one, one);
        exp_range<2, 5>(ebv, zb);
        SB();
        mfma_new(za, ef, cw[0]);
        exp_range<5, 8>(ebv, zb);
        SB();
        mfma_acc(za, stf[0], pb[0][0]);
        arrived4(qf);
        dma16x4(qsrc, vo, qb);
        dma_e(eo, t + 4);
        exp_range<8, 11>(ebv, zb);
        SB();
        mfma_acc(za, stf[1], pb[0][1]);
        exp_range<11, 14>(ebv, zb);
        SB();
        mfma_acc(za, qf[0], sel[0]);
        exp_range<14, 16>(ebv, zb);
        SB();
        rcp_range<0, 1>(ea);
        SB();
        mfma_acc(ebv, one, one);
        rcp_range<1, 4>(ea);
        SB();
        mfma_acc(za, qf[1], sel[1]);
        rcp_range<4, 7>(ea);
        SB();
        mfma_new(zb, ef, cw[1]);
        rcp_range<7, 10>(ea);
        SB();
        mfma_acc(zb, stf[0], pb[1][0]);
        rcp_range<10, 13>(ea);
        SB();
        mfma_acc(zb, stf[1], pb[1][1]);
        rcp_range<13, 16>(ea);
        SB();
        mul16(ea, vf);
        SB();
        __builtin_amdgcn_s_setprio(1);
        mfma_new(vf, vfr[2], sel[0]);
        cvt16(m0, m1, ea);
        SB();
        mfma_acc(vf, vfr[3], sel[1]);
        rcp_range<0, 4>(ebv);
        SB();
        mfma_acc(agg[0], sf[0], __builtin_bit_cast(bf16x8, m0));
        rcp_range<4, 8>(ebv);
        SB();
        mfma_acc(agg[0], sf[1], __builtin_bit_cast(bf16x8, m1));
        rcp_range<8, 12>(ebv);
        SB();
        mfma_acc(zb, qf[2], sel[0]);
        rcp_range<12, 16>(ebv);
        SB();
        mul16(ebv, vf);
        SB();
      }
      __builtin_amdgcn_s_setprio(0);
      cvt16(m0, m1, ebv);
      LSTAMP(ws, 1, 5);
      LCYC(ws, 1, 10);
      agg[1] = mfma(sf[0], __builtin_bit_cast(bf16x8, m0), agg[1]);
      agg[1] = mfma(sf[1], __builtin_bit_cast(bf16x8, m1), agg[1]);
      s_write(sa, (u16)0);
      wait_vm0();
    }
    if (pass + 1 < npass) wg_barrier();
  }
  STAMP(ws, 1, 6);
  LSTAMP(ws, 1, 2);

  {
    const int lane = fresh_lane(), h = lane >> 5;
    const int blk = (lane >> 4) & 1, qq = (lane & 15) >> 2, p = lane & 3;
#pragma unroll
    for (int ct = 0; ct < 2; ++ct)
#pragma unroll
      for (int s = 0; s < 2; ++s) {
        s16x4 part[2];
#pragma unroll
        for (int t = 0; t < 2; ++t) {
          const int row = 16 * s + 8 * h + 4 * t + qq;
          const int ch = 4 * ct + 2 * blk + (p >> 1);
          part[t] = lds_read_tr(vb + row * 128 + ((ch ^ ((row >> 1) & 7)) << 4) + (p & 1) * 8);
        }
        agg[ct] = mfma(sel[s], join8(part[0], part[1]), agg[ct]);
      }
  }
  {
  const int lane = fresh_lane(), h = lane >> 5, f = lane & 31;
  const u32 obase = qb + 4 * h * 128 + f * 2;
#pragma unroll
  for (int ct = 0; ct < 2; ++ct) {
    f32x2 a[6], g[6];
#pragma unroll
    for (int i = 0; i < 6; ++i) {
      a[i] = (f32x2){agg[ct][2 * i], agg[ct][2 * i + 1]};
      const f32x2 y = a[i] * (f32x2){NLOG2E, NLOG2E};
      const f32x2 d = (f32x2){__builtin_amdgcn_exp2f(y.x), __builtin_amdgcn_exp2f(y.y)} + (f32x2){1.0f, 1.0f};
      g[i] = (f32x2){__builtin_amdgcn_rcpf(d.x), __builtin_amdgcn_rcpf(d.y)};
    }
#pragma unroll
    for (int i = 0; i < 6; ++i) {
      const int r = 2 * i;
      const f32x2 o = a[i] * g[i];
      const u32 pk = pack_bf16(o.x, o.y);
      const u32 ad = obase + ((r & 3) + 8 * (r >> 2)) * 128 + ct * 64;
      *(LDSP u16*)(size_t)ad = (u16)pk;
      *(LDSP u16*)(size_t)(ad + 128) = (u16)(pk >> 16);
    }
  }
  mem_fence();
  {
    const rsrc_t outr = make_rsrc(out, (u32)N * D * 2);
#pragma unroll
    for (int i = 0; i < 3; ++i) {
      const int row = 8 * i + (lane >> 3);
      const u32x4 v = lds_read16u(qb + row * 128 + (lane & 7) * 16);
      if (row < tsz)
        __builtin_amdgcn_raw_buffer_store_b128(v, outr, (u32)(n0 + row) * (D * 2) + slice * 128 + (lane & 7) * 16, 0, OUT_AUX);
    }
  }
  }
  STAMP(ws, 1, 7);
  LSTAMP(ws, 1, 3);
  STAMP(ws, 1, 8);
}

inline void launch_k1(void* const* d_in, void* d_ws, hipStream_t stream) {
  (void)hipFuncSetAttribute((const void*)prep_kernel, hipFuncAttributeMaxDynamicSharedMemorySize, K1_LDS);
  prep_kernel<<<NPROD + NGEMM, 512, K1_LDS, stream>>>((const char*)d_in[0], (const char*)d_in[1], (const int*)d_in[2],
                                                    (const int*)d_in[3], (const char*)d_in[4], (const char*)d_in[5],
                                                    (const char*)d_in[6], (char*)d_ws);
}
inline void launch_k2(void* const* d_in, void* d_out, void* d_ws, hipStream_t stream) {
  constexpr u32 K2_DYN = K2_LDS;
  (void)hipFuncSetAttribute((const void*)edge_kernel, hipFuncAttributeMaxDynamicSharedMemorySize, K2_DYN);
  edge_kernel<<<256, 512, K2_DYN, stream>>>((const char*)d_ws, (u16*)d_out);
}

}

extern "C" void kernel_launch(void* const* d_in, const int* in_sizes, int n_in, void* d_out, int out_size,
                              void* d_ws, size_t ws_size, hipStream_t stream) {
  egc::launch_k1(d_in, d_ws, stream);
  egc::launch_k2(d_in, d_out, d_ws, stream);
}
```

```cpp
#include <hip/hip_runtime.h>
#include <hip/hip_bf16.h>
#include <stdint.h>

namespace egc {

typedef unsigned int u32;
typedef unsigned short u16;
typedef float f32x2 __attribute__((ext_vector_type(2)));
typedef float f32x16 __attribute__((ext_vector_type(16)));
typedef __bf16 bf16x2 __attribute__((ext_vector_type(2)));
typedef __bf16 bf16x4 __attribute__((ext_vector_type(4)));
typedef __bf16 bf16x8 __attribute__((ext_vector_type(8)));
typedef short s16x4 __attribute__((ext_vector_type(4)));
typedef u32 u32x2 __attribute__((ext_vector_type(2)));
typedef u32 u32x4 __attribute__((ext_vector_type(4)));
#define LDSP __attribute__((address_space(3)))

constexpr int N = 10000, D = 256, DE = 16, E = 160000;
constexpr int NPROD = 64;
constexpr int CHUNK = E / NPROD;
constexpr int NTILE = 512;
constexpr int TPG = 8;
constexpr int OFFP = 520;
constexpr int NROWP = 10016;
constexpr size_t SLICEB = (size_t)NROWP * 256;
constexpr int CAPT = 448;
constexpr float NLOG2E = -1.44269504088896340736f;

constexpr size_t WS_QV = 0;
constexpr size_t WS_PU = WS_QV + 4 * SLICEB;
constexpr size_t WS_EP = WS_PU + 4 * SLICEB;
constexpr size_t WS_REC = WS_EP + (size_t)(E + 64) * 32;
constexpr size_t WS_OFF = WS_REC + (size_t)(E + 64) * 4;
constexpr int NGROUP = NTILE / TPG;
constexpr int CAPG = 64;
constexpr size_t WS_SLOT = (WS_OFF + (size_t)NPROD * OFFP * 2 + 255) & ~(size_t)255;
constexpr size_t WS_CWF = WS_SLOT + (size_t)NGROUP * NPROD * CAPG * 4;
constexpr size_t WS_END = WS_CWF + 4 * 2 * 64 * 16;

template <int V>
struct IC {
  static constexpr int value = V;
};

__device__ __forceinline__ int tile_start(int t) { return (625 * t) >> 5; }
__device__ __forceinline__ int tile_of(int d) { return (32 * d + 31) / 625; }

__device__ __forceinline__ f32x16 mfma(bf16x8 a, bf16x8 b, f32x16 c) {
  return __builtin_amdgcn_mfma_f32_32x32x16_bf16(a, b, c, 0, 0, 0);
}
__device__ __forceinline__ u32 pack_bf16(float a, float b) {
  bf16x2 v;
  v[0] = (__bf16)a;
  v[1] = (__bf16)b;
  return __builtin_bit_cast(u32, v);
}
__device__ __forceinline__ float bf16_bits_to_f32(u16 b) { return __builtin_bit_cast(float, (u32)b << 16); }

__device__ __forceinline__ void dma16(const void* sbase, u32 voff, u32 lds) {
  asm volatile("s_mov_b32 m0, %2\n\ts_nop 0\n\tglobal_load_lds_dwordx4 %0, %1"
               :
               : "v"(voff), "s"(sbase), "s"(lds)
               : "memory");
}
typedef __amdgpu_buffer_rsrc_t rsrc_t;

#define NT_IN 0
#define XTOUCH 1
#define XSPREAD 0
#define WARM_MODE 1
#define TBL_AUX 16
#define EP_AUX 16
#define OUT_AUX 16
__device__ __forceinline__ rsrc_t make_rsrc(const void* base, u32 bytes) {
  return __builtin_amdgcn_make_buffer_rsrc((void*)base, 0, bytes, 0x00020000);
}
__device__ __forceinline__ void store16_wt(rsrc_t r, u32 off, u32x4 v) {
  __builtin_amdgcn_raw_buffer_store_b128(v, r, off, 0, 16);
}
__device__ __forceinline__ void store4_wt(rsrc_t r, u32 off, u32 v) {
  __builtin_amdgcn_raw_buffer_store_b32(v, r, off, 0, 16);
}
__device__ __forceinline__ int fresh_lane() {
  u32 z;
  asm volatile("s_mov_b32 %0, 0" : "=s"(z));
  return (int)__builtin_amdgcn_mbcnt_hi(~0u, __builtin_amdgcn_mbcnt_lo(~0u, z));
}
__device__ __forceinline__ void bdma16x4(rsrc_t r, u32 v0, u32 v1, u32 v2, u32 v3, u32 soff, u32 lds) {
  asm volatile(
      "s_mov_b32 m0, %6\n\ts_nop 0\n\t"
      "buffer_load_dwordx4 %0, %4, %5 offen lds\n\t"
      "buffer_load_dwordx4 %1, %4, %5 offen offset:1024 lds\n\t"
      "buffer_load_dwordx4 %2, %4, %5 offen offset:2048 lds\n\t"
      "buffer_load_dwordx4 %3, %4, %5 offen offset:3072 lds"
      :
      : "v"(v0), "v"(v1), "v"(v2), "v"(v3), "s"(r), "s"(soff), "s"(lds)
      : "memory");
}
__device__ __forceinline__ void bdma16(rsrc_t r, u32 v, u32 soff, u32 lds) {
  asm volatile("s_mov_b32 m0, %3\n\ts_nop 0\n\tbuffer_load_dwordx4 %0, %1, %2 offen lds"
               :
               : "v"(v), "s"(r), "s"(soff), "s"(lds)
               : "memory");
}
__device__ __forceinline__ void mem_fence() { asm volatile("" ::: "memory"); }
__device__ __forceinline__ void wait_vm0() { asm volatile("s_waitcnt vmcnt(0)" ::: "memory"); }
__device__ __forceinline__ void wait_lgkm0() { asm volatile("s_waitcnt lgkmcnt(0)" ::: "memory"); }
__device__ __forceinline__ void wg_barrier() {
  asm volatile("s_waitcnt lgkmcnt(0)\n\ts_barrier" ::: "memory");
}
__device__ __forceinline__ bf16x8 lds_read16(u32 addr) { return *(const LDSP bf16x8*)(size_t)addr; }
__device__ __forceinline__ u32x4 lds_read16u(u32 addr) { return *(const LDSP u32x4*)(size_t)addr; }
__device__ __forceinline__ s16x4 lds_read_tr(u32 addr) {
  return __builtin_amdgcn_ds_read_tr16_b64_v4i16((LDSP s16x4*)(size_t)addr);
}
__device__ __forceinline__ bf16x8 join8(s16x4 lo, s16x4 hi) {
  u32x2 a = __builtin_bit_cast(u32x2, lo), b = __builtin_bit_cast(u32x2, hi);
  u32x4 r = {a.x, a.y, b.x, b.y};
  return __builtin_bit_cast(bf16x8, r);
}

#define STAMP(wsp, kid, i)
#define CSTAMP(wsp, kid, i, extra)
#define LSTAMP(wsp, kid, i)
#define LCYC(wsp, kid, i)

constexpr int K1_LDS = 163840;
constexpr int NGEMM = 192;

__device__ __forceinline__ u32 wave_scan(u32 v) {
  v += __builtin_amdgcn_update_dpp(0u, v, 0x111, 0xF, 0xF, false);
  v += __builtin_amdgcn_update_dpp(0u, v, 0x112, 0xF, 0xF, false);
  v += __builtin_amdgcn_update_dpp(0u, v, 0x114, 0xF, 0xF, false);
  v += __builtin_amdgcn_update_dpp(0u, v, 0x118, 0xF, 0xF, false);
  v += __builtin_amdgcn_update_dpp(0u, v, 0x142, 0xA, 0xF, false);
  v += __builtin_amdgcn_update_dpp(0u, v, 0x143, 0xC, 0xF, false);
  return v;
}

__device__ __forceinline__ void producer_block(char* smem, int pb, const int* __restrict__ src,
                                               const int* __restrict__ dst, const char* __restrict__ eattr,
                                               const char* __restrict__ gw, char* __restrict__ ws) {
  u32* hist = (u32*)smem;
  u32* wsum = hist + 512;
  u32* srec = wsum + 16;
  u16* seid = (u16*)(srec + 2560);
  u16* sgrp = seid + 2560;
  constexpr u32 EL = 32768;
  const int tid = threadIdx.x, lane = tid & 63, w = tid >> 6;
  const int e0 = pb * CHUNK;
  const rsrc_t wsr = make_rsrc(ws, (u32)WS_END);
  STAMP(ws, 0, 0);
  int dd[5], ss[5];
#pragma unroll
  for (int k = 0; k < 5; ++k) {
    const int i = tid + 512 * k;
    const int ii = i < CHUNK ? i : 0;
    dd[k] = dst[e0 + ii];
    ss[k] = src[e0 + ii];
  }
  hist[tid] = 0;
  wg_barrier();
  LSTAMP(ws, 0, 2);
  u32 rec[5], tl[5], rk[5];
#pragma unroll
  for (int k = 0; k < 5; ++k) {
    const int i = tid + 512 * k;
    const int t = tile_of(dd[k]);
    const u32 dl = dd[k] - tile_start(t);
    rec[k] = ((u32)ss[k] << 8) | (((dl >> 2) & 3) << 22) | (dl << 24) | ((u32)(t & 7) << 29);
    tl[k] = t;
    rk[k] = 0;
    if (i < CHUNK) rk[k] = atomicAdd(&hist[t], 1u);
  }
  {
    const char* ein = eattr + (size_t)e0 * 32;
    const u32 lds0 = (u32)(size_t)(LDSP char*)smem;
    const int wu = __builtin_amdgcn_readfirstlane(w);
#pragma unroll
    for (int k = 0; k < 10; ++k) {
      const int i = tid + 512 * k;
      dma16(ein, (u32)(i < 2 * CHUNK ? i : 0) * 16u, lds0 + EL + (wu * 64 + 512 * k) * 16);
    }
  }
  wg_barrier();
  const u32 c = hist[tid];
  const u32 inc = wave_scan(c);
  if (lane == 63) wsum[w] = inc;
  wg_barrier();
  u32 wb = 0;
#pragma unroll
  for (int j = 0; j < 8; ++j) wb += j < w ? wsum[j] : 0u;
  const u32 base = wb + inc - c;
  hist[tid] = base;
  u16* off = (u16*)(ws + WS_OFF) + pb * OFFP;
  off[tid] = (u16)base;
  if (tid < OFFP - 512) off[512 + tid] = (u16)CHUNK;
  wg_barrier();
#pragma unroll
  for (int k = 0; k < 5; ++k) {
    const int i = tid + 512 * k;
    if (i < CHUNK) {
      const u32 p = hist[tl[k]] + rk[k];
      srec[p] = rec[k];
      seid[p] = (u16)i;
      sgrp[p] = (u16)(tl[k] >> 3);
    }
  }
  LSTAMP(ws, 0, 3);
  wait_vm0();
  wg_barrier();
  LSTAMP(ws, 0, 4);
#pragma unroll
  for (int k = 0; k < 10; ++k) {
    const int i = tid + 512 * k;
    if (i < 2 * CHUNK)
      __builtin_amdgcn_raw_buffer_store_b128(*(const u32x4*)(smem + EL + (int)seid[i >> 1] * 32 + (i & 1) * 16), wsr,
                                             (u32)WS_EP + (u32)e0 * 32u + i * 16, 0, EP_AUX);
  }
#pragma unroll
  for (int k = 0; k < 5; ++k) {
    const int p = tid + 512 * k;
    if (p < CHUNK) {
      const u32 g = sgrp[p], rel = p - hist[g * 8];
      if (rel < (u32)CAPG) store4_wt(wsr, (u32)WS_SLOT + ((g * NPROD + pb) * CAPG + rel) * 4u, srec[p]);
    }
  }
  if (tid < CHUNK / 4) store16_wt(wsr, (u32)WS_REC + (u32)e0 * 4u + tid * 16, *(const u32x4*)(srec + tid * 4));
  if (tid + 512 < CHUNK / 4)
    store16_wt(wsr, (u32)WS_REC + (u32)e0 * 4u + (tid + 512) * 16, *(const u32x4*)(srec + (tid + 512) * 4));
  STAMP(ws, 0, 3);
  if (pb == 1) {
    const int sl = tid >> 7, ct = (tid >> 6) & 1, hh = lane >> 5, ff = lane & 31;
    u32x4 v;
#pragma unroll
    for (int j = 0; j < 4; ++j) {
      const u16* wrow = (const u16*)gw + (512 + 8 * hh + 2 * j) * D + sl * 64 + ct * 32 + ff;
      v[j] = pack_bf16(bf16_bits_to_f32(wrow[0]) * NLOG2E, bf16_bits_to_f32(wrow[D]) * NLOG2E);
    }
    store16_wt(wsr, (u32)WS_CWF + tid * 16, v);
  }
  if (pb == 0) {
    const u32x4 z = {0, 0, 0, 0};
    if (tid < 64) *(u32x4*)(ws + WS_QV + (tid >> 4) * SLICEB + (size_t)N * 256 + (tid & 15) * 16) = z;
    if (tid >= 64 && tid < 66) *(u32x4*)(ws + WS_EP + (size_t)E * 32 + (tid - 64) * 16) = z;
  }
}

__device__ __forceinline__ void gemm_block(char* smem, int gb, const char* __restrict__ x,
                                           const char* __restrict__ uw, const char* __restrict__ vw,
                                           const char* __restrict__ gw, char* __restrict__ ws) {
  const int tid = threadIdx.x, lane = tid & 63;
  const int w = __builtin_amdgcn_readfirstlane(tid >> 6);
  const u32 lds0 = (u32)(size_t)(LDSP char*)smem;
  const int q = gb >> 3, xcd = gb & 7;
  const int cg = q / 3;
  const int type = cg & 3, half = cg >> 2;
  const int rp0 = (q - 3 * cg) * 8 + xcd;
  const int cw4 = w & 3, rg = w >> 2;
  const char* W = (type == 0 ? gw + 256 * 512 : type == 1 ? vw : type == 2 ? gw : uw) + half * 256;
  const float scale = (type & 1) ? 1.0f : NLOG2E;
  const int ctg = half * 4 + cw4;
  const rsrc_t xr = make_rsrc(x, (u32)N * 512u);
  const rsrc_t dr = make_rsrc(ws + (type < 2 ? WS_QV : WS_PU) + (type & 1) * 128 + (size_t)(ctg >> 1) * SLICEB,
                              (u32)N * 256u - 128u);

  constexpr u32 TB = 131072;
  const u32 xlane = (u32)((lane >> 5) * 512) + ((u32)(((lane & 31) ^ (lane >> 5)) << 4) ^ (u32)((cw4 & 1) << 7));
  auto x_soff = [&](int job) { return (u32)((rp0 + 24 * job) * 64 + rg * 32) * 512u + (u32)cw4 * 4096u; };
  auto x_lds = [&](int job) { return lds0 + ((job + 2) & 3) * 32768 + rg * 16384 + cw4 * 4096; };
  auto load_x = [&](int job) { bdma16x4(xr, xlane, xlane ^ 32u, xlane ^ 64u, xlane ^ 96u, x_soff(job), x_lds(job)); };
  STAMP(ws, 0, 0);
  auto load_w = [&](int q) {
#pragma unroll
    for (int i = 0; i < 2; ++i) {
      const int j = 16 * q + 2 * w + i;
      const int k = 4 * j + (lane >> 4);
      const u32 voff = (u32)k * 512u + (u32)(((lane & 15) ^ ((k & 3) << 2)) * 16);
      dma16(W, voff, lds0 + j * 1024);
    }
  };
  load_w(0);
  load_x(0);
  load_w(1);
  load_w(2);
  load_w(3);
  load_x(1);
  u32 xt;
  {
    const int idx = cg * 128 + w * 16 + (lane & 15);
    const u32 off = (u32)((rp0 + 24 * (3 + (idx >> 8))) * 64) * 512u + (u32)(idx & 255) * 128u;
    asm volatile("buffer_load_dword %0, %1, %2, 0 offen" : "=&v"(xt) : "v"(off), "s"(xr) : "memory");
  }

  const int h = lane >> 5, blk = (lane >> 4) & 1, qq = (lane & 15) >> 2, p = lane & 3;
  const int n = lane & 31;
  u32 xaddr[8];
#pragma unroll
  for (int c = 0; c < 8; ++c) xaddr[c] = lds0 + rg * 16384 + n * 512 + (((2 * c + h) ^ (n & 15)) << 4);
  auto xfrag = [&](int job, int ks) { return lds_read16(xaddr[ks & 7] + ((job + 2) & 3) * 32768 + (ks >> 3) * 256); };
  const u32 tb = lds0 + TB + (w >> 1) * 8192;
  const int cpar = w & 1;
  const u32 tput = tb + n * 128, tsw = (4 * cpar + 2 * h) ^ (n & 7);
  const u32 tget = tb + (16 * cpar + (lane >> 3)) * 128 + (((lane & 7) ^ (lane >> 3)) << 4);
  const u32 slane = (u32)((lane >> 3) * 256 + (lane & 7) * 16);
  f32x16 accs[2];
  bf16x8 xf[8], wf[16];

  {
    const int col0 = cw4 * 32 + 16 * (p & 1) + 8 * blk + 4 * (p >> 1);
    const u32 a0 = lds0 + (8 * h + qq) * 256 + ((((col0 >> 3) ^ (qq << 2)) << 4) | ((col0 & 7) << 1));
    auto quarter = [&](auto qc, auto nyoung) {
      constexpr int q = decltype(qc)::value;
      asm volatile("s_waitcnt vmcnt(%0)" ::"n"(decltype(nyoung)::value) : "memory");
      wg_barrier();
      if (q == 2) load_x(2);
#pragma unroll
      for (int ks = 4 * q; ks < 4 * q + 4; ++ks) {
        wf[ks] = join8(lds_read_tr(a0 + ks * 4096), lds_read_tr(a0 + ks * 4096 + 1024));
        xf[ks & 7] = xfrag(0, ks);
      }
      if (q == 3) {
#pragma unroll
        for (int ks = 0; ks < 4; ++ks) xf[ks] = xfrag(1, ks);
      }
      __builtin_amdgcn_sched_barrier(0);
#pragma unroll
      for (int ks = 4 * q; ks < 4 * q + 4; ++ks) {
        if (ks == 0) {
          const f32x16 zero = {0};
          accs[0] = mfma(wf[0], xf[0], zero);
        } else {
          accs[0] = mfma(wf[ks], xf[ks & 7], accs[0]);
        }
      }
      asm volatile("" : "+v"(accs[0]));
      __builtin_amdgcn_sched_barrier(0);
    };
    quarter(IC<0>{}, IC<10 + XTOUCH>{});
    LSTAMP(ws, 0, 2);
    quarter(IC<1>{}, IC<8 + XTOUCH>{});
    quarter(IC<2>{}, IC<6 + XTOUCH>{});
    quarter(IC<3>{}, IC<4 + XTOUCH>{});
    LSTAMP(ws, 0, 4);
    LCYC(ws, 0, 8);
  }

  auto job_body = [&](auto jc) {
    constexpr int job = decltype(jc)::value;
    f32x16& acc = accs[job & 1];
    const f32x16& accp = accs[(job + 1) & 1];
    u32x4 c0, c1, o[2];
    constexpr int young = job == 1 ? 0 : job == 2 ? 4 : job == 3 ? 6 : job == 4 ? 8 : 4;
    if (job <= 5) asm volatile("s_waitcnt vmcnt(%0)" ::"n"(young) : "memory");
    if (job == 1) asm volatile("" ::"v"(xt));
    wg_barrier();
    const u32 xs = x_soff(job + 3), xl = x_lds(job + 3);
    const u32 ds = (u32)((rp0 + 24 * (job - 2)) * 64 + rg * 32 + 16 * cpar) * 256u;
    auto gap = [&](auto kc) {
      constexpr int ks = decltype(kc)::value;
      if (job < 7) {
        if (ks == 0) {
          const f32x16 zero = {0};
          acc = mfma(wf[0], xf[0], zero);
        } else {
          acc = mfma(wf[ks], xf[ks & 7], acc);
        }
        asm volatile("" : "+v"(acc));
        __builtin_amdgcn_sched_barrier(0);
        if (ks < 12) xf[(ks + 4) & 7] = xfrag(job, ks + 4);
        else if (job < 6) xf[(ks + 4) & 7] = xfrag(job + 1, ks - 12);
      }
      if (job >= 1 && job <= 7) {
        if (ks >= 1 && ks <= 4) {
          constexpr int i = ks - 1;
          c0[i] = pack_bf16(accp[2 * i] * scale, accp[2 * i + 1] * scale);
          c1[i] = pack_bf16(accp[8 + 2 * i] * scale, accp[8 + 2 * i + 1] * scale);
        }
        if (ks == 5) {
          const u32 t = tput + ((job - 1) & 1) * 4096;
          *(LDSP u32x4*)(size_t)(t + (tsw << 4)) = c0;
          *(LDSP u32x4*)(size_t)(t + ((tsw ^ 1) << 4)) = c1;
        }
      }
      if (job >= 2 && ks == 0) {
#pragma unroll
        for (int i = 0; i < 2; ++i) o[i] = lds_read16u(tget + ((job - 2) & 1) * 4096 + i * 1024);
      }
      if (job == 1 && ks < 4)
        bdma16(xr, (xlane ^ (u32)(ks << 5)) + ks * 1024, x_soff(3), x_lds(3) + ks * 1024);
      constexpr int k0 = job == 1 ? 4 : 0;
      if (job <= 3 && ks >= k0 && ks <= k0 + 3)
        bdma16(xr, (xlane ^ (u32)((ks - k0) << 5)) + (ks - k0) * 1024, xs, xl + (ks - k0) * 1024);
      constexpr int si = ks == 10 ? 0 : ks == 11 ? 1 : -1;
      if (job >= 2 && si >= 0) {
        __builtin_amdgcn_raw_buffer_store_b128(o[si & 1], dr, slane + (si & 1) * 2048, ds, TBL_AUX);
      }
      __builtin_amdgcn_sched_barrier(0);
    };
    gap(IC<0>{}); gap(IC<1>{}); gap(IC<2>{}); gap(IC<3>{}); gap(IC<4>{}); gap(IC<5>{}); gap(IC<6>{}); gap(IC<7>{});
    gap(IC<8>{}); gap(IC<9>{}); gap(IC<10>{}); gap(IC<11>{}); gap(IC<12>{}); gap(IC<13>{}); gap(IC<14>{});
    gap(IC<15>{});
    if (job == 3) LSTAMP(ws, 0, 5);
    if (job == 6) { LSTAMP(ws, 0, 6); LCYC(ws, 0, 9); }
  };
  job_body(IC<1>{}); job_body(IC<2>{}); job_body(IC<3>{}); job_body(IC<4>{}); job_body(IC<5>{}); job_body(IC<6>{});
  {
    const u32 d6 = lds0 + 98304 + (w >> 1) * 4096 - tb;
    const f32x16& al = accs[0];
    u32x4 c0, c1, o[4];
#pragma unroll
    for (int i = 0; i < 4; ++i) {
      c0[i] = pack_bf16(al[2 * i] * scale, al[2 * i + 1] * scale);
      c1[i] = pack_bf16(al[8 + 2 * i] * scale, al[8 + 2 * i + 1] * scale);
    }
    *(LDSP u32x4*)(size_t)(tput + d6 + (tsw << 4)) = c0;
    *(LDSP u32x4*)(size_t)(tput + d6 + ((tsw ^ 1) << 4)) = c1;
    wg_barrier();
#pragma unroll
    for (int i = 0; i < 2; ++i) {
      o[i] = lds_read16u(tget + 4096 + i * 1024);
      o[2 + i] = lds_read16u(tget + d6 + i * 1024);
    }
    const u32 ds5 = (u32)((rp0 + 24 * 5) * 64 + rg * 32 + 16 * cpar) * 256u;
    const u32 ds6 = (u32)((rp0 + 24 * 6) * 64 + rg * 32 + 16 * cpar) * 256u;
#pragma unroll
    for (int i = 0; i < 2; ++i) {
      __builtin_amdgcn_raw_buffer_store_b128(o[i], dr, slane + i * 2048, ds5, TBL_AUX);
      __builtin_amdgcn_raw_buffer_store_b128(o[2 + i], dr, slane + i * 2048, ds6, TBL_AUX);
    }
  }
}

__global__ void __launch_bounds__(512, 2)
prep_kernel(const char* __restrict__ x, const char* __restrict__ eattr, const int* __restrict__ src,
            const int* __restrict__ dst, const char* __restrict__ uw, const char* __restrict__ vw,
            const char* __restrict__ gw, char* __restrict__ ws) {
  extern __shared__ __attribute__((aligned(16))) char smem[];
  const int b = blockIdx.x;
  LSTAMP(ws, 0, 0);
  if (b < NPROD)
    producer_block(smem, b, src, dst, eattr, gw, ws);
  else
    gemm_block(smem, b - NPROD, x, uw, vw, gw, ws);
  LSTAMP(ws, 0, 1);
}

constexpr u32 L_QV = 0;
constexpr u32 L_E = 65536;
constexpr u32 L_S = L_E + 8 * 4096;
constexpr u32 L_A1 = L_S + 8 * 2048 + 128;
constexpr int LDUMMY = TPG * CAPT;
constexpr u32 L_A2 = L_A1 + (LDUMMY + 16) * 4;
constexpr u32 L_DL = L_A2 + (LDUMMY + 16) * 8;
constexpr u32 L_GS = L_DL + 8 * 64 * 8;
constexpr u32 L_GE = L_GS + 256;
constexpr u32 L_TT = L_GE + 256;
constexpr u32 L_PROG = L_TT + 64;
constexpr u32 L_JUNK = L_PROG + 64;
constexpr u32 K2_LDS = L_JUNK + 256;
static_assert(K2_LDS <= 163840, "LDS budget");

#define SB() __builtin_amdgcn_sched_barrier(0)
__device__ __forceinline__ void mfma_acc(f32x16& c, bf16x8 a, bf16x8 b) {
  c = mfma(a, b, c);
  asm volatile("" : "+v"(c));
  SB();
}
__device__ __forceinline__ void mfma_new(f32x16& c, bf16x8 a, bf16x8 b) {
  const f32x16 zero = {0};
  c = mfma(a, b, zero);
  asm volatile("" : "+v"(c));
  SB();
}
__device__ __forceinline__ void arrived4(const bf16x8 (&v)[4]) {
  asm volatile("" ::"v"(v[0]), "v"(v[1]), "v"(v[2]), "v"(v[3]) : "memory");
}
__device__ __forceinline__ void dma16x4(const void* sbase, const u32 (&vo)[4], u32 lds) {
  asm volatile(
      "s_mov_b32 m0, %5\n\ts_nop 0\n\t"
      "global_load_lds_dwordx4 %0, %4\n\t"
      "global_load_lds_dwordx4 %1, %4 offset:1024\n\t"
      "global_load_lds_dwordx4 %2, %4 offset:2048\n\t"
      "global_load_lds_dwordx4 %3, %4 offset:3072"
      :
      : "v"(vo[0]), "v"(vo[1]), "v"(vo[2]), "v"(vo[3]), "s"(sbase), "s"(lds)
      : "memory");
}
template <int A, int B>
__device__ __forceinline__ void exp_range(f32x16& d, const f32x16& s) {
#pragma unroll
  for (int i = A; i < B; ++i) d[i] = __builtin_amdgcn_exp2f(s[i]);
}
template <int A, int B>
__device__ __forceinline__ void rcp_range(f32x16& d) {
#pragma unroll
  for (int i = A; i < B; ++i) d[i] = __builtin_amdgcn_rcpf(d[i]);
}
__device__ __forceinline__ void mul16(f32x16& g, const f32x16& v) {
#pragma unroll
  for (int i = 0; i < 8; ++i) {
    const f32x2 a = (f32x2){g[2 * i], g[2 * i + 1]} * (f32x2){v[2 * i], v[2 * i + 1]};
    g[2 * i] = a.x;
    g[2 * i + 1] = a.y;
  }
  asm volatile("" : "+v"(g));
}
__device__ __forceinline__ void cvt16(u32x4& m0, u32x4& m1, const f32x16& d) {
#pragma unroll
  for (int i = 0; i < 4; ++i) {
    m0[i] = pack_bf16(d[2 * i], d[2 * i + 1]);
    m1[i] = pack_bf16(d[8 + 2 * i], d[8 + 2 * i + 1]);
  }
  asm volatile("" : "+v"(m0), "+v"(m1));
}
template <int NV>
__device__ __forceinline__ void wait_vm() { asm volatile("s_waitcnt vmcnt(%0)" ::"n"(NV) : "memory"); }
template <int NV>
__device__ __forceinline__ void wait_lgkm() { asm volatile("s_waitcnt lgkmcnt(%0)" ::"n"(NV) : "memory"); }

__global__ void __launch_bounds__(512, 2)
edge_kernel(const char* __restrict__ ws, u16* __restrict__ out) {
  extern __shared__ __attribute__((aligned(16))) char smem[];
  const int tid = threadIdx.x, lane = tid & 63;
  const int w = __builtin_amdgcn_readfirstlane(tid >> 6);
  const u32 lds0 = (u32)(size_t)(LDSP char*)smem;
  const int b = blockIdx.x, xcd = b & 7;
  const int slice = xcd & 3, group = (b >> 3) * 2 + (xcd >> 2);
  const int h = lane >> 5, f = lane & 31;
  STAMP(ws, 1, 0);
  LSTAMP(ws, 1, 0);

  const int pbk = tid >> 3, pj = tid & 7;
  u32 rec0[8], o0, o1;
  u32x4 cwr[2];
  {
    const char* sbase = ws + WS_SLOT + (size_t)group * NPROD * CAPG * 4;
    const u32 so = (u32)(pbk * CAPG + pj) * 4u;
    asm volatile(
        "global_load_dword %0, %8, %9\n\tglobal_load_dword %1, %8, %9 offset:32\n\t"
        "global_load_dword %2, %8, %9 offset:64\n\tglobal_load_dword %3, %8, %9 offset:96\n\t"
        "global_load_dword %4, %8, %9 offset:128\n\tglobal_load_dword %5, %8, %9 offset:160\n\t"
        "global_load_dword %6, %8, %9 offset:192\n\tglobal_load_dword %7, %8, %9 offset:224"
        : "=&v"(rec0[0]), "=&v"(rec0[1]), "=&v"(rec0[2]), "=&v"(rec0[3]), "=&v"(rec0[4]), "=&v"(rec0[5]),
          "=&v"(rec0[6]), "=&v"(rec0[7])
        : "v"(so), "s"(sbase)
        : "memory");
    const char* obase = ws + WS_OFF + (group * TPG + w) * 2;
    const u32 oo = (u32)lane * (OFFP * 2);
    asm volatile("global_load_ushort %0, %2, %3\n\tglobal_load_ushort %1, %2, %3 offset:2"
                 : "=&v"(o0), "=&v"(o1)
                 : "v"(oo), "s"(obase)
                 : "memory");
    const char* cbase = ws + WS_CWF + slice * 2048;
    const u32 co = (u32)lane * 16u;
    asm volatile("global_load_dwordx4 %0, %2, %3\n\tglobal_load_dwordx4 %1, %2, %3 offset:1024"
                 : "=&v"(cwr[0]), "=&v"(cwr[1])
                 : "v"(co), "s"(cbase)
                 : "memory");
  }
  {
    const u32x4 z = {0, 0, 0, 0};
    *(LDSP u32x4*)(size_t)(lds0 + L_S + w * 2048 + lane * 16) = z;
    *(LDSP u32x4*)(size_t)(lds0 + L_S + w * 2048 + 1024 + lane * 16) = z;
  }
  bf16x8 cw[2], sel[2], one;
#pragma unroll
  for (int s = 0; s < 2; ++s)
#pragma unroll
    for (int j = 0; j < 8; ++j) sel[s][j] = (__bf16)(f == 16 * s + 8 * h + j ? 1.0f : 0.0f);
#pragma unroll
  for (int j = 0; j < 8; ++j) one[j] = (__bf16)((j == 0 && h == 0) ? 1.0f : 0.0f);
  u32 cq[4];
#pragma unroll
  for (int j = 0; j < 4; ++j) cq[j] = (u32)(((lane & 7) ^ (4 * (j & 1) + (lane >> 4))) * 16 + 3072 - 1024 * j);
  u32 ce = (u32)(((lane & 1) ^ ((lane >> 4) & 1)) * 16);
  u32 qaddr[4], saddr[2], staddr[2], eaddr, a1, a2, paddr;
  {
    const int sr = (f & 0x13) | ((f & 4) << 1) | ((f & 8) >> 1);
    const int sq = ((sr & 7) << 2) | (sr >> 3);
    const int fsw = (sr >> 1) & 7;
    eaddr = lds0 + L_E + sq * 32 + ((h ^ ((sq >> 3) & 1)) << 4);
#pragma unroll
    for (int c = 0; c < 4; ++c) qaddr[c] = lds0 + L_QV + sr * 128 + (((2 * c + h) ^ fsw) << 4);
    const int blk = (lane >> 4) & 1, qq = (lane & 15) >> 2, p = lane & 3;
    const int c0 = 16 * blk + 8 * (p & 1) + 4 * (p >> 1);
#pragma unroll
    for (int s = 0; s < 2; ++s) {
      saddr[s] = lds0 + L_S + f * 64 + (((2 * s + h) ^ ((f >> 2) & 3)) << 4);
      const int nu = 8 * s + 4 * h + qq;
      staddr[s] = lds0 + L_S + nu * 64 + (((c0 >> 3) ^ ((nu >> 2) & 3)) << 4) + ((c0 & 7) << 1);
    }
    a1 = lds0 + L_A1 + (lane >> 3) * 16;
    a2 = lds0 + L_A2 + (lane >> 1) * 8;
    paddr = lds0 + L_QV + (4 * h + qq) * 128 + (16 * blk + 4 * p) * 2;
  }
  asm volatile("" : "+v"(sel[0]), "+v"(sel[1]), "+v"(one), "+v"(cq[0]), "+v"(cq[1]), "+v"(cq[2]), "+v"(cq[3]), "+v"(ce));
  asm volatile("" : "+v"(qaddr[0]), "+v"(qaddr[1]), "+v"(qaddr[2]), "+v"(qaddr[3]), "+v"(saddr[0]), "+v"(saddr[1]),
               "+v"(staddr[0]), "+v"(staddr[1]), "+v"(eaddr), "+v"(a1), "+v"(a2), "+v"(paddr));
  SB();
  asm volatile("s_waitcnt vmcnt(0)"
               : "+v"(rec0[0]), "+v"(rec0[1]), "+v"(rec0[2]), "+v"(rec0[3]), "+v"(rec0[4]), "+v"(rec0[5]),
                 "+v"(rec0[6]), "+v"(rec0[7]), "+v"(o0), "+v"(o1), "+v"(cwr[0]), "+v"(cwr[1])
               :
               : "memory");
  SB();
  LSTAMP(ws, 1, 11);
  {
    const int tile = group * TPG + w;
    const int n0w = tile_start(tile), tszw = tile_start(tile + 1) - n0w;
    const char* pu = ws + WS_PU + (size_t)slice * SLICEB;
    const u32 rb = lds0 + L_QV + w * 8192;
#pragma unroll
    for (int j = 0; j < 4; ++j) {
      const int row = 8 * j + (lane >> 3);
      const int node = n0w + (row < tszw ? row : tszw - 1);
      const u32 voff = (u32)node * 256u + (lane & 7) * 16;
      dma16(pu, voff, rb + j * 1024);
    }
    constexpr int ROWS = NROWP / 32;
    const int idx = w * 64 + lane;
    const u32 row = (u32)(b >> 3) * ROWS + (u32)(idx < ROWS ? idx : ROWS - 1);
    asm volatile("s_mov_b32 m0, %2\n\ts_nop 0\n\tglobal_load_lds_dword %0, %1"
                 :
                 : "v"(row * 256u), "s"(ws + WS_QV + (size_t)slice * SLICEB), "s"(lds0 + L_JUNK)
                 : "memory");
  }
  cw[0] = __builtin_bit_cast(bf16x8, cwr[0]);
  cw[1] = __builtin_bit_cast(bf16x8, cwr[1]);
  const int cnt = (int)o1 - (int)o0;
  const int inc = (int)wave_scan((u32)cnt);
  u32x2* dl = (u32x2*)(smem + L_DL);
  {
    const int delta = inc - cnt - (int)o0;
    dl[w * 64 + lane] = (u32x2){(u32)(delta + w * CAPT), (u32)min((int)o1, CAPT - delta)};
  }
  if (w == 0) ((int*)(smem + L_GS))[lane] = (int)o0;
  if (w == 7) ((int*)(smem + L_GE))[lane] = (int)o1;
  if (lane == 63) ((int*)(smem + L_TT))[w] = inc;
  STAMP(ws, 1, 1);
  wg_barrier();
  STAMP(ws, 1, 2);
  LSTAMP(ws, 1, 6);

  const int* tt = (const int*)(smem + L_TT);
  int tau, mytot, maxt;
  {
    const int ti = tt[lane >> 3], tj = tt[lane & 7];
    const bool before = ti > tj || (ti == tj && (lane >> 3) < (lane & 7));
    const unsigned long long m = __ballot(before);
    const int rank = __popcll(m & (0x0101010101010101ull << (lane & 7)));
    const int want = w < 4 ? w : 11 - w;
    tau = __builtin_ctzll(__ballot(lane < 8 && rank == want));
    mytot = __builtin_amdgcn_readlane(tj, tau);
    maxt = __builtin_amdgcn_readlane(tj, __builtin_ctzll(__ballot(lane < 8 && rank == 0)));
  }
  const int npass = maxt > CAPT ? (maxt + CAPT - 1) / CAPT : 1;
  const int tile = group * TPG + tau;
  const int n0 = tile_start(tile), tsz = tile_start(tile + 1) - n0;
  const u32 qb = lds0 + L_QV + tau * 8192, vb = qb + 4096;
  const u32* grec = (const u32*)(ws + WS_REC);

  const char* qsrc = ws + WS_QV + (size_t)slice * SLICEB - 3072;
  const char* vsrc = qsrc + 128;
  const char* esrc = ws + WS_EP;
  const u32 eb = lds0 + L_E + tau * 4096;
  const u32 simg = lds0 + L_S;
  eaddr += tau * 4096;
#pragma unroll
  for (int c = 0; c < 4; ++c) qaddr[c] += tau * 8192;
#pragma unroll
  for (int s = 0; s < 2; ++s) {
    saddr[s] += tau * 2048;
    staddr[s] += tau * 2048;
  }
  a1 += tau * (CAPT * 4);
  a2 += tau * (CAPT * 8);
  paddr += tau * 8192;
  auto read_st = [&](int s) { return join8(lds_read_tr(staddr[0] + s * 1024), lds_read_tr(staddr[1] + s * 1024)); };
  STAMP(ws, 1, 3);
  LSTAMP(ws, 1, 7);

  auto build_lists = [&](int pass, bool first) {
    u32* A1 = (u32*)(smem + L_A1);
    u32x2* A2 = (u32x2*)(smem + L_A2);
    const int tb = threadIdx.x >> 3, tj = threadIdx.x & 7;
    const int bs0 = ((const int*)(smem + L_GS))[tb], be0 = ((const int*)(smem + L_GE))[tb];
    const u32x2* dlb = dl + tb;
    const int pos0 = tb * CHUNK;
    auto put = [&](u32 rec, int i, u32 li, bool ok) {
      const u32 idx = ok ? li : (u32)(LDUMMY + (threadIdx.x & 15));
      A1[idx] = rec & 0x3FFF00u;
      const u32 sa = ((rec >> 18) & 0x3FF0u) ^ (((li & 3) << 4) | ((li >> 1) & 0xE));
      A2[idx] = (u32x2){(u32)(pos0 + i) << 5, sa};
    };
    int i0 = bs0 + tj;
    if (first) {
      u32x2 d[8];
#pragma unroll
      for (int k = 0; k < 6; ++k) d[k] = dlb[(rec0[k] >> 29) * 64];
#pragma unroll
      for (int k = 0; k < 6; ++k) {
        const int i = i0 + 8 * k;
        put(rec0[k], i, (u32)i + d[k].x, i < (int)d[k].y);
      }
#pragma unroll
      for (int k = 6; k < 8; ++k) {
        const int i = i0 + 8 * k;
        if (__any(i < be0)) {
          const u32x2 dk = dlb[(rec0[k] >> 29) * 64];
          put(rec0[k], i, (u32)i + dk.x, i < (int)dk.y);
        }
      }
      i0 += CAPG;
    }
    for (int i = i0; i < be0; i += 8) {
      const u32 rec = grec[pos0 + i];
      const u32 t = rec >> 29;
      const u32 rel = (u32)i + dlb[t * 64].x - t * CAPT - (u32)(pass * CAPT);
      put(rec, i, t * CAPT + rel, rel < (u32)CAPT);
    }
    int tw = ((const int*)(smem + L_TT))[w] - pass * CAPT;
    tw = tw < 0 ? 0 : tw;
    const int ln = threadIdx.x & 63;
    if (tw < CAPT && ln < 32) {
      const int li = (tw & ~31) + ln;
      if (li >= tw) {
        A1[w * CAPT + li] = (u32)N * 256u;
        A2[w * CAPT + li] = (u32x2){(u32)E * 32u, 8u * 2048u};
      }
    }
  };
  build_lists(0, true);
  LSTAMP(ws, 1, 12);
  wait_vm<1>();
  LSTAMP(ws, 1, 13);
  STAMP(ws, 1, 4);
  wg_barrier();
  bf16x8 pb[2][2];
#pragma unroll
  for (int ct = 0; ct < 2; ++ct)
#pragma unroll
    for (int s = 0; s < 2; ++s) {
      const u32 a = paddr + s * 2048 + ct * 64;
      pb[ct][s] = join8(lds_read_tr(a), lds_read_tr(a + 8 * 128));
    }
  f32x16 agg[2] = {{0}, {0}};
  auto u_offsets = [&](u32 (&o)[4]) {
    const int ln = fresh_lane();
#pragma unroll
    for (int j = 0; j < 4; ++j) {
      const int row = 8 * j + (ln >> 3);
      o[j] = (u32)(WS_PU - WS_QV) + (u32)(n0 + (row < tsz ? row : tsz - 1)) * 256u + cq[j];
    }
  };

  for (int pass = 0; pass < npass; ++pass) {
    if (pass > 0) {
      build_lists(pass, false);
      wg_barrier();
    }
    STAMP(ws, 1, 5);
    LSTAMP(ws, 1, 1);

    int rem = mytot - pass * CAPT;
    rem = rem < 0 ? 0 : (rem > CAPT ? CAPT : rem);
    const int nsteps = __builtin_amdgcn_readfirstlane(rem > 0 ? (rem + 31) >> 5 : 1);
    {
      const int last = nsteps - 1;
      auto clampt = [&](int t) { return t < last ? t : last; };
      auto rd16 = [&](int t) -> u32 { return *(const LDSP u32*)(size_t)(a2 + t * 256 + 4); };
      auto rd32 = [&](int t) -> u32 { return *(const LDSP u32*)(size_t)(a2 + t * 256); };
      auto s_write = [&](u32 a, u16 v) { *(LDSP u16*)(size_t)(simg + a) = v; };
      auto dma_e = [&](u32 eo, int t) { dma16(esrc, eo + ce, eb + (t & 3) * 1024); };
      u32 vo[4];
      auto set_vo = [&](const u32x4& so) {
#pragma unroll
        for (int j = 0; j < 4; ++j) vo[j] = so[j] + cq[j];
      };
      const f32x16 zero = {0};

      __builtin_amdgcn_s_setprio(3);
      const u32x4 so0 = lds_read16u(a1);
      const u32 eo0 = rd32(0), eo1 = rd32(clampt(1)), eo2 = rd32(clampt(2)), eo3 = rd32(clampt(3));
      u32 sa = rd16(0);
      u32 sa1 = rd16(clampt(1));
      const u32x4 so1 = lds_read16u(a1 + clampt(1) * 128);
      asm volatile("" ::"v"(pb[0][0]), "v"(pb[0][1]), "v"(pb[1][0]), "v"(pb[1][1]) : "memory");
      SB();
      set_vo(so0);
      dma_e(eo0, 0);
      dma16x4(qsrc, vo, qb);
      dma16x4(vsrc, vo, vb);
      dma_e(eo1, 1);
      dma_e(eo2, 2);
      s_write(sa, (u16)0x3F80);
      mem_fence();
      bf16x8 stf[2], sf[2], qf[4], vfr[4], ef;
#pragma unroll
      for (int s = 0; s < 2; ++s) stf[s] = read_st(s);
      LSTAMP(ws, 1, 14);
      wait_vm<6>();
      LSTAMP(ws, 1, 15);
      ef = lds_read16(eaddr);
#pragma unroll
      for (int c = 0; c < 4; ++c) qf[c] = lds_read16(qaddr[c]);
      set_vo(so1);
      arrived4(qf);
      dma16x4(qsrc, vo, qb);
      f32x16 za, zb, ea, ebv = zero, vf;
      za = mfma(ef, cw[0], zero);
      zb = mfma(ef, cw[1], zero);
      za = mfma(stf[0], pb[0][0], za);
      zb = mfma(stf[0], pb[1][0], zb);
      za = mfma(stf[1], pb[0][1], za);
      zb = mfma(stf[1], pb[1][1], zb);
      za = mfma(qf[0], sel[0], za);
      zb = mfma(qf[2], sel[0], zb);
      za = mfma(qf[1], sel[1], za);
      u32x4 m0 = {0, 0, 0, 0}, m1 = {0, 0, 0, 0};
      sf[0] = __builtin_bit_cast(bf16x8, m0);
      sf[1] = sf[0];
      dma_e(eo3, 3);
      SB();

      LSTAMP(ws, 1, 4);
      LCYC(ws, 1, 9);
      __builtin_amdgcn_s_setprio(1);
      for (int t = 0; t <= last; ++t) {
        wait_vm<5>();
#pragma unroll
        for (int c = 0; c < 4; ++c) vfr[c] = lds_read16(qaddr[c] + 4096);
        SB();
        const u32x4 so = lds_read16u(a1 + clampt(t + 2) * 128);
        const u32 eo = rd32(clampt(t + 4));
        const u32 sa2 = rd16(clampt(t + 2));
        SB();
        mfma_acc(zb, qf[3], sel[1]);
        cvt16(m0, m1, ebv);
        SB();
        mfma_acc(agg[1], sf[0], __builtin_bit_cast(bf16x8, m0));
        exp_range<0, 4>(ea, za);
        SB();
        mfma_acc(agg[1], sf[1], __builtin_bit_cast(bf16x8, m1));
        sf[0] = lds_read16(saddr[0]);
        sf[1] = lds_read16(saddr[1]);
        mem_fence();
        s_write(sa, (u16)0);
        s_write(sa1, (u16)0x3F80);
        mem_fence();
        stf[0] = read_st(0);
        stf[1] = read_st(1);
        exp_range<4, 8>(ea, za);
        SB();
        mfma_new(vf, vfr[0], sel[0]);
        ef = lds_read16(eaddr + ((t + 1) & 3) * 1024);
        exp_range<8, 12>(ea, za);
        SB();
        mfma_acc(vf, vfr[1], sel[1]);
        arrived4(vfr);
        if (t == last && pass + 1 == npass) u_offsets(vo);
        dma16x4(vsrc, vo, vb);
        wait_vm<5>();
#pragma unroll
        for (int c = 0; c < 4; ++c) qf[c] = lds_read16(qaddr[c]);
        sa = sa1;
        sa1 = sa2;
        exp_range<12, 16>(ea, za);
        SB();
        __builtin_amdgcn_s_setprio(0);
        set_vo(so);
        exp_range<0, 2>(ebv, zb);
        SB();
        mfma_acc(ea, one, one);
        exp_range<2, 5>(ebv, zb);
        SB();
        mfma_new(za, ef, cw[0]);
        exp_range<5, 8>(ebv, zb);
        SB();
        mfma_acc(za, stf[0], pb[0][0]);
        arrived4(qf);
        dma16x4(qsrc, vo, qb);
        dma_e(eo, t + 4);
        exp_range<8, 11>(ebv, zb);
        SB();
        mfma_acc(za, stf[1], pb[0][1]);
        exp_range<11, 14>(ebv, zb);
        SB();
        mfma_acc(za, qf[0], sel[0]);
        exp_range<14, 16>(ebv, zb);
        SB();
        rcp_range<0, 1>(ea);
        SB();
        mfma_acc(ebv, one, one);
        rcp_range<1, 4>(ea);
        SB();
        mfma_acc(za, qf[1], sel[1]);
        rcp_range<4, 7>(ea);
        SB();
        mfma_new(zb, ef, cw[1]);
        rcp_range<7, 10>(ea);
        SB();
        mfma_acc(zb, stf[0], pb[1][0]);
        rcp_range<10, 13>(ea);
        SB();
        mfma_acc(zb, stf[1], pb[1][1]);
        rcp_range<13, 16>(ea);
        SB();
        mul16(ea, vf);
        SB();
        __builtin_amdgcn_s_setprio(1);
        mfma_new(vf, vfr[2], sel[0]);
        cvt16(m0, m1, ea);
        SB();
        mfma_acc(vf, vfr[3], sel[1]);
        rcp_range<0, 4>(ebv);
        SB();
        mfma_acc(agg[0], sf[0], __builtin_bit_cast(bf16x8, m0));
        rcp_range<4, 8>(ebv);
        SB();
        mfma_acc(agg[0], sf[1], __builtin_bit_cast(bf16x8, m1));
        rcp_range<8, 12>(ebv);
        SB();
        mfma_acc(zb, qf[2], sel[0]);
        rcp_range<12, 16>(ebv);
        SB();
        mul16(ebv, vf);
        SB();
      }
      __builtin_amdgcn_s_setprio(0);
      cvt16(m0, m1, ebv);
      LSTAMP(ws, 1, 5);
      LCYC(ws, 1, 10);
      agg[1] = mfma(sf[0], __builtin_bit_cast(bf16x8, m0), agg[1]);
      agg[1] = mfma(sf[1], __builtin_bit_cast(bf16x8, m1), agg[1]);
      s_write(sa, (u16)0);
      wait_vm0();
    }
    if (pass + 1 < npass) wg_barrier();
  }
  STAMP(ws, 1, 6);
  LSTAMP(ws, 1, 2);

  {
    const int lane = fresh_lane(), h = lane >> 5;
    const int blk = (lane >> 4) & 1, qq = (lane & 15) >> 2, p = lane & 3;
#pragma unroll
    for (int ct = 0; ct < 2; ++ct)
#pragma unroll
      for (int s = 0; s < 2; ++s) {
        s16x4 part[2];
#pragma unroll
        for (int t = 0; t < 2; ++t) {
          const int row = 16 * s + 8 * h + 4 * t + qq;
          const int ch = 4 * ct + 2 * blk + (p >> 1);
          part[t] = lds_read_tr(vb + row * 128 + ((ch ^ ((row >> 1) & 7)) << 4) + (p & 1) * 8);
        }
        agg[ct] = mfma(sel[s], join8(part[0], part[1]), agg[ct]);
      }
  }
  {
  const int lane = fresh_lane(), h = lane >> 5, f = lane & 31;
  const u32 obase = qb + 4 * h * 128 + f * 2;
#pragma unroll
  for (int ct = 0; ct < 2; ++ct) {
    f32x2 a[6], g[6];
#pragma unroll
    for (int i = 0; i < 6; ++i) {
      a[i] = (f32x2){agg[ct][2 * i], agg[ct][2 * i + 1]};
      const f32x2 y = a[i] * (f32x2){NLOG2E, NLOG2E};
      const f32x2 d = (f32x2){__builtin_amdgcn_exp2f(y.x), __builtin_amdgcn_exp2f(y.y)} + (f32x2){1.0f, 1.0f};
      g[i] = (f32x2){__builtin_amdgcn_rcpf(d.x), __builtin_amdgcn_rcpf(d.y)};
    }
#pragma unroll
    for (int i = 0; i < 6; ++i) {
      const int r = 2 * i;
      const f32x2 o = a[i] * g[i];
      const u32 pk = pack_bf16(o.x, o.y);
      const u32 ad = obase + ((r & 3) + 8 * (r >> 2)) * 128 + ct * 64;
      *(LDSP u16*)(size_t)ad = (u16)pk;
      *(LDSP u16*)(size_t)(ad + 128) = (u16)(pk >> 16);
    }
  }
  mem_fence();
  {
    const rsrc_t outr = make_rsrc(out, (u32)N * D * 2);
#pragma unroll
    for (int i = 0; i < 3; ++i) {
      const int row = 8 * i + (lane >> 3);
      const u32x4 v = lds_read16u(qb + row * 128 + (lane & 7) * 16);
      if (row < tsz)
        __builtin_amdgcn_raw_buffer_store_b128(v, outr, (u32)(n0 + row) * (D * 2) + slice * 128 + (lane & 7) * 16, 0, OUT_AUX);
    }
  }
  }
  STAMP(ws, 1, 7);
  LSTAMP(ws, 1, 3);
  STAMP(ws, 1, 8);
}

inline void launch_k1(void* const* d_in, void* d_ws, hipStream_t stream) {
  (void)hipFuncSetAttribute((const void*)prep_kernel, hipFuncAttributeMaxDynamicSharedMemorySize, K1_LDS);
  prep_kernel<<<NPROD + NGEMM, 512, K1_LDS, stream>>>((const char*)d_in[0], (const char*)d_in[1], (const int*)d_in[2],
                                                    (const int*)d_in[3], (const char*)d_in[4], (const char*)d_in[5],
                                                    (const char*)d_in[6], (char*)d_ws);
}
inline void launch_k2(void* const* d_in, void* d_out, void* d_ws, hipStream_t stream) {
  constexpr u32 K2_DYN = K2_LDS;
  (void)hipFuncSetAttribute((const void*)edge_kernel, hipFuncAttributeMaxDynamicSharedMemorySize, K2_DYN);
  edge_kernel<<<256, 512, K2_DYN, stream>>>((const char*)d_ws, (u16*)d_out);
}

}

extern "C" void kernel_launch(void* const* d_in, const int* in_sizes, int n_in, void* d_out, int out_size,
                              void* d_ws, size_t ws_size, hipStream_t stream) {
  egc::launch_k1(d_in, d_ws, stream);
  egc::launch_k2(d_in, d_out, d_ws, stream);
}
```

```cpp
#include <hip/hip_runtime.h>
#include <hip/hip_bf16.h>
#include <stdint.h>

namespace egc {

typedef unsigned int u32;
typedef unsigned short u16;
typedef float f32x2 __attribute__((ext_vector_type(2)));
typedef float f32x16 __attribute__((ext_vector_type(16)));
typedef __bf16 bf16x2 __attribute__((ext_vector_type(2)));
typedef __bf16 bf16x8 __attribute__((ext_vector_type(8)));
typedef short s16x4 __attribute__((ext_vector_type(4)));
typedef u32 u32x2 __attribute__((ext_vector_type(2)));
typedef u32 u32x4 __attribute__((ext_vector_type(4)));
#define LDSP __attribute__((address_space(3)))

constexpr int N = 10000, D = 256, DE = 16, E = 160000;
constexpr int NPROD = 64;
constexpr int CHUNK = E / NPROD;
constexpr int NTILE = 512;
constexpr int TPG = 8;
constexpr int OFFP = 520;
constexpr int NROWP = 10016;
constexpr size_t SLICEB = (size_t)NROWP * 256;
constexpr int CAPT = 448;
constexpr float NLOG2E = -1.44269504088896340736f;

constexpr size_t WS_QV = 0;
constexpr size_t WS_PU = WS_QV + 4 * SLICEB;
constexpr size_t WS_EP = WS_PU + 4 * SLICEB;
constexpr size_t WS_REC = WS_EP + (size_t)(E + 64) * 32;
constexpr size_t WS_OFF = WS_REC + (size_t)(E + 64) * 4;
constexpr int NGROUP = NTILE / TPG;
constexpr int CAPG = 64;
constexpr size_t WS_SLOT = (WS_OFF + (size_t)NPROD * OFFP * 2 + 255) & ~(size_t)255;
constexpr size_t WS_CWF = WS_SLOT + (size_t)NGROUP * NPROD * CAPG * 4;
constexpr size_t WS_END = WS_CWF + 4 * 2 * 64 * 16;

template <int V>
struct IC {
  static constexpr int value = V;
};

__device__ __forceinline__ int tile_start(int t) { return (625 * t) >> 5; }
__device__ __forceinline__ int tile_of(int d) { return (32 * d + 31) / 625; }

__device__ __forceinline__ f32x16 mfma(bf16x8 a, bf16x8 b, f32x16 c) {
  return __builtin_amdgcn_mfma_f32_32x32x16_bf16(a, b, c, 0, 0, 0);
}
__device__ __forceinline__ u32 pack_bf16(float a, float b) {
  bf16x2 v;
  v[0] = (__bf16)a;
  v[1] = (__bf16)b;
  return __builtin_bit_cast(u32, v);
}
__device__ __forceinline__ float bf16_bits_to_f32(u16 b) { return __builtin_bit_cast(float, (u32)b << 16); }

__device__ __forceinline__ void dma16(const void* sbase, u32 voff, u32 lds) {
  asm volatile("s_mov_b32 m0, %2\n\ts_nop 0\n\tglobal_load_lds_dwordx4 %0, %1"
               :
               : "v"(voff), "s"(sbase), "s"(lds)
               : "memory");
}
typedef __amdgpu_buffer_rsrc_t rsrc_t;
__device__ __forceinline__ rsrc_t make_rsrc(const void* base, u32 bytes) {
  return __builtin_amdgcn_make_buffer_rsrc((void*)base, 0, bytes, 0x00020000);
}
__device__ __forceinline__ void store16_wt(rsrc_t r, u32 off, u32x4 v) {
  __builtin_amdgcn_raw_buffer_store_b128(v, r, off, 0, 16);
}
__device__ __forceinline__ void store4_wt(rsrc_t r, u32 off, u32 v) {
  __builtin_amdgcn_raw_buffer_store_b32(v, r, off, 0, 16);
}
__device__ __forceinline__ int fresh_lane() {
  u32 z;
  asm volatile("s_mov_b32 %0, 0" : "=s"(z));
  return (int)__builtin_amdgcn_mbcnt_hi(~0u, __builtin_amdgcn_mbcnt_lo(~0u, z));
}
__device__ __forceinline__ void bdma16x4(rsrc_t r, u32 v0, u32 v1, u32 v2, u32 v3, u32 soff, u32 lds) {
  asm volatile(
      "s_mov_b32 m0, %6\n\ts_nop 0\n\t"
      "buffer_load_dwordx4 %0, %4, %5 offen lds\n\t"
      "buffer_load_dwordx4 %1, %4, %5 offen offset:1024 lds\n\t"
      "buffer_load_dwordx4 %2, %4, %5 offen offset:2048 lds\n\t"
      "buffer_load_dwordx4 %3, %4, %5 offen offset:3072 lds"
      :
      : "v"(v0), "v"(v1), "v"(v2), "v"(v3), "s"(r), "s"(soff), "s"(lds)
      : "memory");
}
__device__ __forceinline__ void bdma16(rsrc_t r, u32 v, u32 soff, u32 lds) {
  asm volatile("s_mov_b32 m0, %3\n\ts_nop 0\n\tbuffer_load_dwordx4 %0, %1, %2 offen lds"
               :
               : "v"(v), "s"(r), "s"(soff), "s"(lds)
               : "memory");
}
__device__ __forceinline__ void mem_fence() { asm volatile("" ::: "memory"); }
__device__ __forceinline__ void wait_vm0() { asm volatile("s_waitcnt vmcnt(0)" ::: "memory"); }
__device__ __forceinline__ void wg_barrier() {
  asm volatile("s_waitcnt lgkmcnt(0)\n\ts_barrier" ::: "memory");
}
__device__ __forceinline__ bf16x8 lds_read16(u32 addr) { return *(const LDSP bf16x8*)(size_t)addr; }
__device__ __forceinline__ u32x4 lds_read16u(u32 addr) { return *(const LDSP u32x4*)(size_t)addr; }
__device__ __forceinline__ s16x4 lds_read_tr(u32 addr) {
  return __builtin_amdgcn_ds_read_tr16_b64_v4i16((LDSP s16x4*)(size_t)addr);
}
__device__ __forceinline__ bf16x8 join8(s16x4 lo, s16x4 hi) {
  u32x2 a = __builtin_bit_cast(u32x2, lo), b = __builtin_bit_cast(u32x2, hi);
  u32x4 r = {a.x, a.y, b.x, b.y};
  return __builtin_bit_cast(bf16x8, r);
}

constexpr int K1_LDS = 163840;
constexpr int NGEMM = 192;

__device__ __forceinline__ u32 wave_scan(u32 v) {
  v += __builtin_amdgcn_update_dpp(0u, v, 0x111, 0xF, 0xF, false);
  v += __builtin_amdgcn_update_dpp(0u, v, 0x112, 0xF, 0xF, false);
  v += __builtin_amdgcn_update_dpp(0u, v, 0x114, 0xF, 0xF, false);
  v += __builtin_amdgcn_update_dpp(0u, v, 0x118, 0xF, 0xF, false);
  v += __builtin_amdgcn_update_dpp(0u, v, 0x142, 0xA, 0xF, false);
  v += __builtin_amdgcn_update_dpp(0u, v, 0x143, 0xC, 0xF, false);
  return v;
}

__device__ __forceinline__ void producer_block(char* smem, int pb, const int* __restrict__ src,
                                               const int* __restrict__ dst, const char* __restrict__ eattr,
                                               const char* __restrict__ gw, char* __restrict__ ws) {
  u32* hist = (u32*)smem;
  u32* wsum = hist + 512;
  u32* srec = wsum + 16;
  u16* seid = (u16*)(srec + 2560);
  u16* sgrp = seid + 2560;
  constexpr u32 EL = 32768;
  const int tid = threadIdx.x, lane = tid & 63, w = tid >> 6;
  const int e0 = pb * CHUNK;
  const rsrc_t wsr = make_rsrc(ws, (u32)WS_END);
  int dd[5], ss[5];
#pragma unroll
  for (int k = 0; k < 5; ++k) {
    const int i = tid + 512 * k;
    const int ii = i < CHUNK ? i : 0;
    dd[k] = dst[e0 + ii];
    ss[k] = src[e0 + ii];
  }
  hist[tid] = 0;
  wg_barrier();
  u32 rec[5], tl[5], rk[5];
#pragma unroll
  for (int k = 0; k < 5; ++k) {
    const int i = tid + 512 * k;
    const int t = tile_of(dd[k]);
    const u32 dl = dd[k] - tile_start(t);
    rec[k] = ((u32)ss[k] << 8) | (((dl >> 2) & 3) << 22) | (dl << 24) | ((u32)(t & 7) << 29);
    tl[k] = t;
    rk[k] = 0;
    if (i < CHUNK) rk[k] = atomicAdd(&hist[t], 1u);
  }
  {
    const char* ein = eattr + (size_t)e0 * 32;
    const u32 lds0 = (u32)(size_t)(LDSP char*)smem;
    const int wu = __builtin_amdgcn_readfirstlane(w);
#pragma unroll
    for (int k = 0; k < 10; ++k) {
      const int i = tid + 512 * k;
      dma16(ein, (u32)(i < 2 * CHUNK ? i : 0) * 16u, lds0 + EL + (wu * 64 + 512 * k) * 16);
    }
  }
  wg_barrier();
  const u32 c = hist[tid];
  const u32 inc = wave_scan(c);
  if (lane == 63) wsum[w] = inc;
  wg_barrier();
  u32 wb = 0;
#pragma unroll
  for (int j = 0; j < 8; ++j) wb += j < w ? wsum[j] : 0u;
  const u32 base = wb + inc - c;
  hist[tid] = base;
  u16* off = (u16*)(ws + WS_OFF) + pb * OFFP;
  off[tid] = (u16)base;
  if (tid < OFFP - 512) off[512 + tid] = (u16)CHUNK;
  wg_barrier();
#pragma unroll
  for (int k = 0; k < 5; ++k) {
    const int i = tid + 512 * k;
    if (i < CHUNK) {
      const u32 p = hist[tl[k]] + rk[k];
      srec[p] = rec[k];
      seid[p] = (u16)i;
      sgrp[p] = (u16)(tl[k] >> 3);
    }
  }
  wait_vm0();
  wg_barrier();
#pragma unroll
  for (int k = 0; k < 10; ++k) {
    const int i = tid + 512 * k;
    if (i < 2 * CHUNK)
      __builtin_amdgcn_raw_buffer_store_b128(*(const u32x4*)(smem + EL + (int)seid[i >> 1] * 32 + (i & 1) * 16), wsr,
                                             (u32)WS_EP + (u32)e0 * 32u + i * 16, 0, 16);
  }
#pragma unroll
  for (int k = 0; k < 5; ++k) {
    const int p = tid + 512 * k;
    if (p < CHUNK) {
      const u32 g = sgrp[p], rel = p - hist[g * 8];
      if (rel < (u32)CAPG) store4_wt(wsr, (u32)WS_SLOT + ((g * NPROD + pb) * CAPG + rel) * 4u, srec[p]);
    }
  }
  if (tid < CHUNK / 4) store16_wt(wsr, (u32)WS_REC + (u32)e0 * 4u + tid * 16, *(const u32x4*)(srec + tid * 4));
  if (tid + 512 < CHUNK / 4)
    store16_wt(wsr, (u32)WS_REC + (u32)e0 * 4u + (tid + 512) * 16, *(const u32x4*)(srec + (tid + 512) * 4));
  if (pb == 1) {
    const int sl = tid >> 7, ct = (tid >> 6) & 1, hh = lane >> 5, ff = lane & 31;
    u32x4 v;
#pragma unroll
    for (int j = 0; j < 4; ++j) {
      const u16* wrow = (const u16*)gw + (512 + 8 * hh + 2 * j) * D + sl * 64 + ct * 32 + ff;
      v[j] = pack_bf16(bf16_bits_to_f32(wrow[0]) * NLOG2E, bf16_bits_to_f32(wrow[D]) * NLOG2E);
    }
    store16_wt(wsr, (u32)WS_CWF + tid * 16, v);
  }
  if (pb == 0) {
    const u32x4 z = {0, 0, 0, 0};
    if (tid < 64) *(u32x4*)(ws + WS_QV + (tid >> 4) * SLICEB + (size_t)N * 256 + (tid & 15) * 16) = z;
    if (tid >= 64 && tid < 66) *(u32x4*)(ws + WS_EP + (size_t)E * 32 + (tid - 64) * 16) = z;
  }
}

__device__ __forceinline__ void gemm_block(char* smem, int gb, const char* __restrict__ x,
                                           const char* __restrict__ uw, const char* __restrict__ vw,
                                           const char* __restrict__ gw, char* __restrict__ ws) {
  const int tid = threadIdx.x, lane = tid & 63;
  const int w = __builtin_amdgcn_readfirstlane(tid >> 6);
  const u32 lds0 = (u32)(size_t)(LDSP char*)smem;
  const int q = gb >> 3, xcd = gb & 7;
  const int cg = q / 3;
  const int type = cg & 3, half = cg >> 2;
  const int rp0 = (q - 3 * cg) * 8 + xcd;
  const int cw4 = w & 3, rg = w >> 2;
  const char* W = (type == 0 ? gw + 256 * 512 : type == 1 ? vw : type == 2 ? gw : uw) + half * 256;
  const float scale = (type & 1) ? 1.0f : NLOG2E;
  const int ctg = half * 4 + cw4;
  const rsrc_t xr = make_rsrc(x, (u32)N * 512u);
  const rsrc_t dr = make_rsrc(ws + (type < 2 ? WS_QV : WS_PU) + (type & 1) * 128 + (size_t)(ctg >> 1) * SLICEB,
                              (u32)N * 256u - 128u);

  constexpr u32 TB = 131072;
  const u32 xlane = (u32)((lane >> 5) * 512) + ((u32)(((lane & 31) ^ (lane >> 5)) << 4) ^ (u32)((cw4 & 1) << 7));
  auto x_soff = [&](int job) { return (u32)((rp0 + 24 * job) * 64 + rg * 32) * 512u + (u32)cw4 * 4096u; };
  auto x_lds = [&](int job) { return lds0 + ((job + 2) & 3) * 32768 + rg * 16384 + cw4 * 4096; };
  auto load_x = [&](int job) { bdma16x4(xr, xlane, xlane ^ 32u, xlane ^ 64u, xlane ^ 96u, x_soff(job), x_lds(job)); };
  auto load_w = [&](int q) {
#pragma unroll
    for (int i = 0; i < 2; ++i) {
      const int j = 16 * q + 2 * w + i;
      const int k = 4 * j + (lane >> 4);
      const u32 voff = (u32)k * 512u + (u32)(((lane & 15) ^ ((k & 3) << 2)) * 16);
      dma16(W, voff, lds0 + j * 1024);
    }
  };
  load_w(0);
  load_x(0);
  load_w(1);
  load_w(2);
  load_w(3);
  load_x(1);
  u32 xt;
  {
    const int idx = cg * 128 + w * 16 + (lane & 15);
    const u32 off = (u32)((rp0 + 24 * (3 + (idx >> 8))) * 64) * 512u + (u32)(idx & 255) * 128u;
    asm volatile("buffer_load_dword %0, %1, %2, 0 offen" : "=&v"(xt) : "v"(off), "s"(xr) : "memory");
  }

  const int h = lane >> 5, blk = (lane >> 4) & 1, qq = (lane & 15) >> 2, p = lane & 3;
  const int n = lane & 31;
  u32 xaddr[8];
#pragma unroll
  for (int c = 0; c < 8; ++c) xaddr[c] = lds0 + rg * 16384 + n * 512 + (((2 * c + h) ^ (n & 15)) << 4);
  auto xfrag = [&](int job, int ks) { return lds_read16(xaddr[ks & 7] + ((job + 2) & 3) * 32768 + (ks >> 3) * 256); };
  const u32 tb = lds0 + TB + (w >> 1) * 8192;
  const int cpar = w & 1;
  const u32 tput = tb + n * 128, tsw = (4 * cpar + 2 * h) ^ (n & 7);
  const u32 tget = tb + (16 * cpar + (lane >> 3)) * 128 + (((lane & 7) ^ (lane >> 3)) << 4);
  const u32 slane = (u32)((lane >> 3) * 256 + (lane & 7) * 16);
  f32x16 accs[2];
  bf16x8 xf[8], wf[16];

  {
    const int col0 = cw4 * 32 + 16 * (p & 1) + 8 * blk + 4 * (p >> 1);
    const u32 a0 = lds0 + (8 * h + qq) * 256 + ((((col0 >> 3) ^ (qq << 2)) << 4) | ((col0 & 7) << 1));
    auto quarter = [&](auto qc, auto nyoung) {
      constexpr int q = decltype(qc)::value;
      asm volatile("s_waitcnt vmcnt(%0)" ::"n"(decltype(nyoung)::value) : "memory");
      wg_barrier();
      if (q == 2) load_x(2);
#pragma unroll
      for (int ks = 4 * q; ks < 4 * q + 4; ++ks) {
        wf[ks] = join8(lds_read_tr(a0 + ks * 4096), lds_read_tr(a0 + ks * 4096 + 1024));
        xf[ks & 7] = xfrag(0, ks);
      }
      if (q == 3) {
#pragma unroll
        for (int ks = 0; ks < 4; ++ks) xf[ks] = xfrag(1, ks);
      }
      __builtin_amdgcn_sched_barrier(0);
#pragma unroll
      for (int ks = 4 * q; ks < 4 * q + 4; ++ks) {
        if (ks == 0) {
          const f32x16 zero = {0};
          accs[0] = mfma(wf[0], xf[0], zero);
        } else {
          accs[0] = mfma(wf[ks], xf[ks & 7], accs[0]);
        }
      }
      asm volatile("" : "+v"(accs[0]));
      __builtin_amdgcn_sched_barrier(0);
    };
    quarter(IC<0>{}, IC<11>{});
    quarter(IC<1>{}, IC<9>{});
    quarter(IC<2>{}, IC<7>{});
    quarter(IC<3>{}, IC<5>{});
  }

  auto job_body = [&](auto jc) {
    constexpr int job = decltype(jc)::value;
    f32x16& acc = accs[job & 1];
    const f32x16& accp = accs[(job + 1) & 1];
    u32x4 c0, c1, o[2];
    constexpr int young = job == 1 ? 0 : job == 2 ? 4 : job == 3 ? 6 : job == 4 ? 8 : 4;
    if (job <= 5) asm volatile("s_waitcnt vmcnt(%0)" ::"n"(young) : "memory");
    if (job == 1) asm volatile("" ::"v"(xt));
    wg_barrier();
    const u32 xs = x_soff(job + 3), xl = x_lds(job + 3);
    const u32 ds = (u32)((rp0 + 24 * (job - 2)) * 64 + rg * 32 + 16 * cpar) * 256u;
    auto gap = [&](auto kc) {
      constexpr int ks = decltype(kc)::value;
      if (ks == 0) {
        const f32x16 zero = {0};
        acc = mfma(wf[0], xf[0], zero);
      } else {
        acc = mfma(wf[ks], xf[ks & 7], acc);
      }
      asm volatile("" : "+v"(acc));
      __builtin_amdgcn_sched_barrier(0);
      if (ks < 12) xf[(ks + 4) & 7] = xfrag(job, ks + 4);
      else if (job < 6) xf[(ks + 4) & 7] = xfrag(job + 1, ks - 12);
      if (ks >= 1 && ks <= 4) {
        constexpr int i = ks - 1;
        c0[i] = pack_bf16(accp[2 * i] * scale, accp[2 * i + 1] * scale);
        c1[i] = pack_bf16(accp[8 + 2 * i] * scale, accp[8 + 2 * i + 1] * scale);
      }
      if (ks == 5) {
        const u32 t = tput + ((job - 1) & 1) * 4096;
        *(LDSP u32x4*)(size_t)(t + (tsw << 4)) = c0;
        *(LDSP u32x4*)(size_t)(t + ((tsw ^ 1) << 4)) = c1;
      }
      if (job >= 2 && ks == 0) {
#pragma unroll
        for (int i = 0; i < 2; ++i) o[i] = lds_read16u(tget + ((job - 2) & 1) * 4096 + i * 1024);
      }
      if (job == 1 && ks < 4)
        bdma16(xr, (xlane ^ (u32)(ks << 5)) + ks * 1024, x_soff(3), x_lds(3) + ks * 1024);
      constexpr int k0 = job == 1 ? 4 : 0;
      if (job <= 3 && ks >= k0 && ks <= k0 + 3)
        bdma16(xr, (xlane ^ (u32)((ks - k0) << 5)) + (ks - k0) * 1024, xs, xl + (ks - k0) * 1024);
      constexpr int si = ks == 10 ? 0 : ks == 11 ? 1 : -1;
      if (job >= 2 && si >= 0) {
        __builtin_amdgcn_raw_buffer_store_b128(o[si & 1], dr, slane + (si & 1) * 2048, ds, 16);
      }
      __builtin_amdgcn_sched_barrier(0);
    };
    gap(IC<0>{}); gap(IC<1>{}); gap(IC<2>{}); gap(IC<3>{}); gap(IC<4>{}); gap(IC<5>{}); gap(IC<6>{}); gap(IC<7>{});
    gap(IC<8>{}); gap(IC<9>{}); gap(IC<10>{}); gap(IC<11>{}); gap(IC<12>{}); gap(IC<13>{}); gap(IC<14>{});
    gap(IC<15>{});
  };
  job_body(IC<1>{}); job_body(IC<2>{}); job_body(IC<3>{}); job_body(IC<4>{}); job_body(IC<5>{}); job_body(IC<6>{});
  {
    const u32 d6 = lds0 + 98304 + (w >> 1) * 4096 - tb;
    const f32x16& al = accs[0];
    u32x4 c0, c1, o[4];
#pragma unroll
    for (int i = 0; i < 4; ++i) {
      c0[i] = pack_bf16(al[2 * i] * scale, al[2 * i + 1] * scale);
      c1[i] = pack_bf16(al[8 + 2 * i] * scale, al[8 + 2 * i + 1] * scale);
    }
    *(LDSP u32x4*)(size_t)(tput + d6 + (tsw << 4)) = c0;
    *(LDSP u32x4*)(size_t)(tput + d6 + ((tsw ^ 1) << 4)) = c1;
    wg_barrier();
#pragma unroll
    for (int i = 0; i < 2; ++i) {
      o[i] = lds_read16u(tget + 4096 + i * 1024);
      o[2 + i] = lds_read16u(tget + d6 + i * 1024);
    }
    const u32 ds5 = (u32)((rp0 + 24 * 5) * 64 + rg * 32 + 16 * cpar) * 256u;
    const u32 ds6 = (u32)((rp0 + 24 * 6) * 64 + rg * 32 + 16 * cpar) * 256u;
#pragma unroll
    for (int i = 0; i < 2; ++i) {
      __builtin_amdgcn_raw_buffer_store_b128(o[i], dr, slane + i * 2048, ds5, 16);
      __builtin_amdgcn_raw_buffer_store_b128(o[2 + i], dr, slane + i * 2048, ds6, 16);
    }
  }
}

__global__ void __launch_bounds__(512, 2)
prep_kernel(const char* __restrict__ x, const char* __restrict__ eattr, const int* __restrict__ src,
            const int* __restrict__ dst, const char* __restrict__ uw, const char* __restrict__ vw,
            const char* __restrict__ gw, char* __restrict__ ws) {
  extern __shared__ __attribute__((aligned(16))) char smem[];
  const int b = blockIdx.x;
  if (b < NPROD)
    producer_block(smem, b, src, dst, eattr, gw, ws);
  else
    gemm_block(smem, b - NPROD, x, uw, vw, gw, ws);
}

constexpr u32 L_QV = 0;
constexpr u32 L_E = 65536;
constexpr u32 L_S = L_E + 8 * 4096;
constexpr u32 L_A1 = L_S + 8 * 2048 + 128;
constexpr int LDUMMY = TPG * CAPT;
constexpr u32 L_A2 = L_A1 + (LDUMMY + 16) * 4;
constexpr u32 L_DL = L_A2 + (LDUMMY + 16) * 8;
constexpr u32 L_GS = L_DL + 8 * 64 * 8;
constexpr u32 L_GE = L_GS + 256;
constexpr u32 L_TT = L_GE + 256;
constexpr u32 L_JUNK = L_TT + 64;
constexpr u32 K2_LDS = L_JUNK + 256;
static_assert(K2_LDS <= 163840, "LDS budget");

#define SB() __builtin_amdgcn_sched_barrier(0)
__device__ __forceinline__ void mfma_acc(f32x16& c, bf16x8 a, bf16x8 b) {
  c = mfma(a, b, c);
  asm volatile("" : "+v"(c));
  SB();
}
__device__ __forceinline__ void mfma_new(f32x16& c, bf16x8 a, bf16x8 b) {
  const f32x16 zero = {0};
  c = mfma(a, b, zero);
  asm volatile("" : "+v"(c));
  SB();
}
__device__ __forceinline__ void arrived4(const bf16x8 (&v)[4]) {
  asm volatile("" ::"v"(v[0]), "v"(v[1]), "v"(v[2]), "v"(v[3]) : "memory");
}
__device__ __forceinline__ void dma16x4(const void* sbase, const u32 (&vo)[4], u32 lds) {
  asm volatile(
      "s_mov_b32 m0, %5\n\ts_nop 0\n\t"
      "global_load_lds_dwordx4 %0, %4\n\t"
      "global_load_lds_dwordx4 %1, %4 offset:1024\n\t"
      "global_load_lds_dwordx4 %2, %4 offset:2048\n\t"
      "global_load_lds_dwordx4 %3, %4 offset:3072"
      :
      : "v"(vo[0]), "v"(vo[1]), "v"(vo[2]), "v"(vo[3]), "s"(sbase), "s"(lds)
      : "memory");
}
template <int A, int B>
__device__ __forceinline__ void exp_range(f32x16& d, const f32x16& s) {
#pragma unroll
  for (int i = A; i < B; ++i) d[i] = __builtin_amdgcn_exp2f(s[i]);
}
template <int A, int B>
__device__ __forceinline__ void rcp_range(f32x16& d) {
#pragma unroll
  for (int i = A; i < B; ++i) d[i] = __builtin_amdgcn_rcpf(d[i]);
}
__device__ __forceinline__ void mul16(f32x16& g, const f32x16& v) {
#pragma unroll
  for (int i = 0; i < 8; ++i) {
    const f32x2 a = (f32x2){g[2 * i], g[2 * i + 1]} * (f32x2){v[2 * i], v[2 * i + 1]};
    g[2 * i] = a.x;
    g[2 * i + 1] = a.y;
  }
  asm volatile("" : "+v"(g));
}
__device__ __forceinline__ void cvt16(u32x4& m0, u32x4& m1, const f32x16& d) {
#pragma unroll
  for (int i = 0; i < 4; ++i) {
    m0[i] = pack_bf16(d[2 * i], d[2 * i + 1]);
    m1[i] = pack_bf16(d[8 + 2 * i], d[8 + 2 * i + 1]);
  }
  asm volatile("" : "+v"(m0), "+v"(m1));
}
template <int NV>
__device__ __forceinline__ void wait_vm() { asm volatile("s_waitcnt vmcnt(%0)" ::"n"(NV) : "memory"); }

__global__ void __launch_bounds__(512, 2)
edge_kernel(const char* __restrict__ ws, u16* __restrict__ out) {
  extern __shared__ __attribute__((aligned(16))) char smem[];
  const int tid = threadIdx.x, lane = tid & 63;
  const int w = __builtin_amdgcn_readfirstlane(tid >> 6);
  const u32 lds0 = (u32)(size_t)(LDSP char*)smem;
  const int b = blockIdx.x, xcd = b & 7;
  const int slice = xcd & 3, group = (b >> 3) * 2 + (xcd >> 2);
  const int h = lane >> 5, f = lane & 31;

  const int pbk = tid >> 3, pj = tid & 7;
  u32 rec0[8], o0, o1;
  u32x4 cwr[2];
  {
    const char* sbase = ws + WS_SLOT + (size_t)group * NPROD * CAPG * 4;
    const u32 so = (u32)(pbk * CAPG + pj) * 4u;
    asm volatile(
        "global_load_dword %0, %8, %9\n\tglobal_load_dword %1, %8, %9 offset:32\n\t"
        "global_load_dword %2, %8, %9 offset:64\n\tglobal_load_dword %3, %8, %9 offset:96\n\t"
        "global_load_dword %4, %8, %9 offset:128\n\tglobal_load_dword %5, %8, %9 offset:160\n\t"
        "global_load_dword %6, %8, %9 offset:192\n\tglobal_load_dword %7, %8, %9 offset:224"
        : "=&v"(rec0[0]), "=&v"(rec0[1]), "=&v"(rec0[2]), "=&v"(rec0[3]), "=&v"(rec0[4]), "=&v"(rec0[5]),
          "=&v"(rec0[6]), "=&v"(rec0[7])
        : "v"(so), "s"(sbase)
        : "memory");
    const char* obase = ws + WS_OFF + (group * TPG + w) * 2;
    const u32 oo = (u32)lane * (OFFP * 2);
    asm volatile("global_load_ushort %0, %2, %3\n\tglobal_load_ushort %1, %2, %3 offset:2"
                 : "=&v"(o0), "=&v"(o1)
                 : "v"(oo), "s"(obase)
                 : "memory");
    const char* cbase = ws + WS_CWF + slice * 2048;
    const u32 co = (u32)lane * 16u;
    asm volatile("global_load_dwordx4 %0, %2, %3\n\tglobal_load_dwordx4 %1, %2, %3 offset:1024"
                 : "=&v"(cwr[0]), "=&v"(cwr[1])
                 : "v"(co), "s"(cbase)
                 : "memory");
  }
  {
    const u32x4 z = {0, 0, 0, 0};
    *(LDSP u32x4*)(size_t)(lds0 + L_S + w * 2048 + lane * 16) = z;
    *(LDSP u32x4*)(size_t)(lds0 + L_S + w * 2048 + 1024 + lane * 16) = z;
  }
  bf16x8 cw[2], sel[2], one;
#pragma unroll
  for (int s = 0; s < 2; ++s)
#pragma unroll
    for (int j = 0; j < 8; ++j) sel[s][j] = (__bf16)(f == 16 * s + 8 * h + j ? 1.0f : 0.0f);
#pragma unroll
  for (int j = 0; j < 8; ++j) one[j] = (__bf16)((j == 0 && h == 0) ? 1.0f : 0.0f);
  u32 cq[4];
#pragma unroll
  for (int j = 0; j < 4; ++j) cq[j] = (u32)(((lane & 7) ^ (4 * (j & 1) + (lane >> 4))) * 16 + 3072 - 1024 * j);
  u32 ce = (u32)(((lane & 1) ^ ((lane >> 4) & 1)) * 16);
  u32 qaddr[4], saddr[2], staddr[2], eaddr, a1, a2, paddr;
  {
    const int sr = (f & 0x13) | ((f & 4) << 1) | ((f & 8) >> 1);
    const int sq = ((sr & 7) << 2) | (sr >> 3);
    const int fsw = (sr >> 1) & 7;
    eaddr = lds0 + L_E + sq * 32 + ((h ^ ((sq >> 3) & 1)) << 4);
#pragma unroll
    for (int c = 0; c < 4; ++c) qaddr[c] = lds0 + L_QV + sr * 128 + (((2 * c + h) ^ fsw) << 4);
    const int blk = (lane >> 4) & 1, qq = (lane & 15) >> 2, p = lane & 3;
    const int c0 = 16 * blk + 8 * (p & 1) + 4 * (p >> 1);
#pragma unroll
    for (int s = 0; s < 2; ++s) {
      saddr[s] = lds0 + L_S + f * 64 + (((2 * s + h) ^ ((f >> 2) & 3)) << 4);
      const int nu = 8 * s + 4 * h + qq;
      staddr[s] = lds0 + L_S + nu * 64 + (((c0 >> 3) ^ ((nu >> 2) & 3)) << 4) + ((c0 & 7) << 1);
    }
    a1 = lds0 + L_A1 + (lane >> 3) * 16;
    a2 = lds0 + L_A2 + (lane >> 1) * 8;
    paddr = lds0 + L_QV + 4096 + (4 * h + qq) * 128 + (16 * blk + 4 * p) * 2;
  }
  asm volatile("" : "+v"(sel[0]), "+v"(sel[1]), "+v"(one), "+v"(cq[0]), "+v"(cq[1]), "+v"(cq[2]), "+v"(cq[3]), "+v"(ce));
  asm volatile("" : "+v"(qaddr[0]), "+v"(qaddr[1]), "+v"(qaddr[2]), "+v"(qaddr[3]), "+v"(saddr[0]), "+v"(saddr[1]),
               "+v"(staddr[0]), "+v"(staddr[1]), "+v"(eaddr), "+v"(a1), "+v"(a2), "+v"(paddr));
  SB();
  asm volatile("s_waitcnt vmcnt(0)"
               : "+v"(rec0[0]), "+v"(rec0[1]), "+v"(rec0[2]), "+v"(rec0[3]), "+v"(rec0[4]), "+v"(rec0[5]),
                 "+v"(rec0[6]), "+v"(rec0[7]), "+v"(o0), "+v"(o1), "+v"(cwr[0]), "+v"(cwr[1])
               :
               : "memory");
  SB();
  {
    const int tile = group * TPG + w;
    const int n0w = tile_start(tile), tszw = tile_start(tile + 1) - n0w;
    const char* pu = ws + WS_PU + (size_t)slice * SLICEB;
    const u32 rb = lds0 + L_QV + w * 8192 + 4096;
#pragma unroll
    for (int j = 0; j < 4; ++j) {
      const int row = 8 * j + (lane >> 3);
      const int node = n0w + (row < tszw ? row : tszw - 1);
      const u32 voff = (u32)node * 256u + (lane & 7) * 16;
      dma16(pu, voff, rb + j * 1024);
    }
    constexpr int ROWS = NROWP / 32;
    const int idx = w * 64 + lane;
    const u32 row = (u32)(b >> 3) * ROWS + (u32)(idx < ROWS ? idx : ROWS - 1);
    asm volatile("s_mov_b32 m0, %2\n\ts_nop 0\n\tglobal_load_lds_dword %0, %1"
                 :
                 : "v"(row * 256u), "s"(ws + WS_QV + (size_t)slice * SLICEB), "s"(lds0 + L_JUNK)
                 : "memory");
  }
  cw[0] = __builtin_bit_cast(bf16x8, cwr[0]);
  cw[1] = __builtin_bit_cast(bf16x8, cwr[1]);
  const int cnt = (int)o1 - (int)o0;
  const int inc = (int)wave_scan((u32)cnt);
  u32x2* dl = (u32x2*)(smem + L_DL);
  {
    const int delta = inc - cnt - (int)o0;
    dl[w * 64 + lane] = (u32x2){(u32)(delta + w * CAPT), (u32)min((int)o1, CAPT - delta)};
  }
  if (w == 0) ((int*)(smem + L_GS))[lane] = (int)o0;
  if (w == 7) ((int*)(smem + L_GE))[lane] = (int)o1;
  if (lane == 63) ((int*)(smem + L_TT))[w] = inc;
  wg_barrier();

  const int* tt = (const int*)(smem + L_TT);
  int tau, mytot, maxt;
  {
    const int ti = tt[lane >> 3], tj = tt[lane & 7];
    const bool before = ti > tj || (ti == tj && (lane >> 3) < (lane & 7));
    const unsigned long long m = __ballot(before);
    const int rank = __popcll(m & (0x0101010101010101ull << (lane & 7)));
    const int want = w < 4 ? w : 11 - w;
    tau = __builtin_ctzll(__ballot(lane < 8 && rank == want));
    mytot = __builtin_amdgcn_readlane(tj, tau);
    maxt = __builtin_amdgcn_readlane(tj, __builtin_ctzll(__ballot(lane < 8 && rank == 0)));
  }
  const int npass = maxt > CAPT ? (maxt + CAPT - 1) / CAPT : 1;
  const int tile = group * TPG + tau;
  const int n0 = tile_start(tile), tsz = tile_start(tile + 1) - n0;
  const u32 qb = lds0 + L_QV + tau * 8192, vb = qb + 4096;
  const u32* grec = (const u32*)(ws + WS_REC);

  const char* qsrc = ws + WS_QV + (size_t)slice * SLICEB - 3072;
  const char* vsrc = qsrc + 128;
  const char* esrc = ws + WS_EP;
  const u32 eb = lds0 + L_E + tau * 4096;
  const u32 simg = lds0 + L_S;
  eaddr += tau * 4096;
#pragma unroll
  for (int c = 0; c < 4; ++c) qaddr[c] += tau * 8192;
#pragma unroll
  for (int s = 0; s < 2; ++s) {
    saddr[s] += tau * 2048;
    staddr[s] += tau * 2048;
  }
  a1 += tau * (CAPT * 4);
  a2 += tau * (CAPT * 8);
  paddr += tau * 8192;
  auto read_st = [&](int s) { return join8(lds_read_tr(staddr[0] + s * 1024), lds_read_tr(staddr[1] + s * 1024)); };

  auto build_lists = [&](int pass, bool first) {
    u32* A1 = (u32*)(smem + L_A1);
    u32x2* A2 = (u32x2*)(smem + L_A2);
    const int tb = threadIdx.x >> 3, tj = threadIdx.x & 7;
    const int bs0 = ((const int*)(smem + L_GS))[tb], be0 = ((const int*)(smem + L_GE))[tb];
    const u32x2* dlb = dl + tb;
    const int pos0 = tb * CHUNK;
    auto put = [&](u32 rec, int i, u32 li, bool ok) {
      const u32 idx = ok ? li : (u32)(LDUMMY + (threadIdx.x & 15));
      A1[idx] = rec & 0x3FFF00u;
      const u32 sa = ((rec >> 18) & 0x3FF0u) ^ (((li & 3) << 4) | ((li >> 1) & 0xE));
      A2[idx] = (u32x2){(u32)(pos0 + i) << 5, sa};
    };
    int i0 = bs0 + tj;
    if (first) {
      u32x2 d[8];
#pragma unroll
      for (int k = 0; k < 6; ++k) d[k] = dlb[(rec0[k] >> 29) * 64];
#pragma unroll
      for (int k = 0; k < 6; ++k) {
        const int i = i0 + 8 * k;
        put(rec0[k], i, (u32)i + d[k].x, i < (int)d[k].y);
      }
#pragma unroll
      for (int k = 6; k < 8; ++k) {
        const int i = i0 + 8 * k;
        if (__any(i < be0)) {
          const u32x2 dk = dlb[(rec0[k] >> 29) * 64];
          put(rec0[k], i, (u32)i + dk.x, i < (int)dk.y);
        }
      }
      i0 += CAPG;
    }
    for (int i = i0; i < be0; i += 8) {
      const u32 rec = grec[pos0 + i];
      const u32 t = rec >> 29;
      const u32 rel = (u32)i + dlb[t * 64].x - t * CAPT - (u32)(pass * CAPT);
      put(rec, i, t * CAPT + rel, rel < (u32)CAPT);
    }
    int tw = ((const int*)(smem + L_TT))[w] - pass * CAPT;
    tw = tw < 0 ? 0 : tw;
    const int ln = threadIdx.x & 63;
    if (tw < CAPT && ln < 32) {
      const int li = (tw & ~31) + ln;
      if (li >= tw) {
        A1[w * CAPT + li] = (u32)N * 256u;
        A2[w * CAPT + li] = (u32x2){(u32)E * 32u, 8u * 2048u};
      }
    }
  };
  build_lists(0, true);
  wait_vm<1>();
  wg_barrier();
  bf16x8 pb[2][2];
#pragma unroll
  for (int ct = 0; ct < 2; ++ct)
#pragma unroll
    for (int s = 0; s < 2; ++s) {
      const u32 a = paddr + s * 2048 + ct * 64;
      pb[ct][s] = join8(lds_read_tr(a), lds_read_tr(a + 8 * 128));
    }
  f32x16 agg[2] = {{0}, {0}};
  auto u_offsets = [&](u32 (&o)[4]) {
    const int ln = fresh_lane();
#pragma unroll
    for (int j = 0; j < 4; ++j) {
      const int row = 8 * j + (ln >> 3);
      o[j] = (u32)(WS_PU - WS_QV) + (u32)(n0 + (row < tsz ? row : tsz - 1)) * 256u + cq[j];
    }
  };

  for (int pass = 0; pass < npass; ++pass) {
    if (pass > 0) {
      build_lists(pass, false);
      wg_barrier();
    }

    int rem = mytot - pass * CAPT;
    rem = rem < 0 ? 0 : (rem > CAPT ? CAPT : rem);
    const int nsteps = __builtin_amdgcn_readfirstlane(rem > 0 ? (rem + 31) >> 5 : 1);
    {
      const int last = nsteps - 1;
      auto clampt = [&](int t) { return t < last ? t : last; };
      auto rd16 = [&](int t) -> u32 { return *(const LDSP u32*)(size_t)(a2 + t * 256 + 4); };
      auto rd32 = [&](int t) -> u32 { return *(const LDSP u32*)(size_t)(a2 + t * 256); };
      auto s_write = [&](u32 a, u16 v) { *(LDSP u16*)(size_t)(simg + a) = v; };
      auto dma_e = [&](u32 eo, int t) { dma16(esrc, eo + ce, eb + (t & 3) * 1024); };
      u32 vo[4];
      auto set_vo = [&](const u32x4& so) {
#pragma unroll
        for (int j = 0; j < 4; ++j) vo[j] = so[j] + cq[j];
      };
      const f32x16 zero = {0};

      __builtin_amdgcn_s_setprio(3);
      const u32x4 so0 = lds_read16u(a1);
      const u32 eo0 = rd32(0);
      u32 sa = rd16(0);
      const u32 eo1 = rd32(clampt(1)), eo2 = rd32(clampt(2)), eo3 = rd32(clampt(3));
      u32 sa1 = rd16(clampt(1));
      const u32x4 so1 = lds_read16u(a1 + clampt(1) * 128);
      SB();
      set_vo(so0);
      dma_e(eo0, 0);
      dma16x4(qsrc, vo, qb);
      asm volatile("" ::"v"(pb[0][0]), "v"(pb[0][1]), "v"(pb[1][0]), "v"(pb[1][1]) : "memory");
      dma16x4(vsrc, vo, vb);
      dma_e(eo1, 1);
      dma_e(eo2, 2);
      s_write(sa, (u16)0x3F80);
      mem_fence();
      bf16x8 stf[2], sf[2], qf[4], vfr[4], ef;
#pragma unroll
      for (int s = 0; s < 2; ++s) stf[s] = read_st(s);
      wait_vm<10>();
      ef = lds_read16(eaddr);
      f32x16 za, zb, ea, ebv = zero, vf;
      za = mfma(ef, cw[0], zero);
      zb = mfma(ef, cw[1], zero);
      za = mfma(stf[0], pb[0][0], za);
      zb = mfma(stf[0], pb[1][0], zb);
      za = mfma(stf[1], pb[0][1], za);
      zb = mfma(stf[1], pb[1][1], zb);
      asm volatile("" : "+v"(za), "+v"(zb));
      SB();
      wait_vm<6>();
#pragma unroll
      for (int c = 0; c < 4; ++c) qf[c] = lds_read16(qaddr[c]);
      set_vo(so1);
      arrived4(qf);
      dma16x4(qsrc, vo, qb);
      za = mfma(qf[0], sel[0], za);
      zb = mfma(qf[2], sel[0], zb);
      za = mfma(qf[1], sel[1], za);
      u32x4 m0 = {0, 0, 0, 0}, m1 = {0, 0, 0, 0};
      sf[0] = __builtin_bit_cast(bf16x8, m0);
      sf[1] = sf[0];
      dma_e(eo3, 3);
      SB();

      __builtin_amdgcn_s_setprio(1);
      for (int t = 0; t <= last; ++t) {
        wait_vm<5>();
#pragma unroll
        for (int c = 0; c < 4; ++c) vfr[c] = lds_read16(qaddr[c] + 4096);
        SB();
        const u32x4 so = lds_read16u(a1 + clampt(t + 2) * 128);
        const u32 eo = rd32(clampt(t + 4));
        const u32 sa2 = rd16(clampt(t + 2));
        SB();
        mfma_acc(zb, qf[3], sel[1]);
        cvt16(m0, m1, ebv);
        SB();
        mfma_acc(agg[1], sf[0], __builtin_bit_cast(bf16x8, m0));
        exp_range<0, 4>(ea, za);
        SB();
        mfma_acc(agg[1], sf[1], __builtin_bit_cast(bf16x8, m1));
        sf[0] = lds_read16(saddr[0]);
        sf[1] = lds_read16(saddr[1]);
        mem_fence();
        s_write(sa, (u16)0);
        s_write(sa1, (u16)0x3F80);
        mem_fence();
        stf[0] = read_st(0);
        stf[1] = read_st(1);
        exp_range<4, 8>(ea, za);
        SB();
        mfma_new(vf, vfr[0], sel[0]);
        ef = lds_read16(eaddr + ((t + 1) & 3) * 1024);
        exp_range<8, 12>(ea, za);
        SB();
        mfma_acc(vf, vfr[1], sel[1]);
        arrived4(vfr);
        if (t == last && pass + 1 == npass) u_offsets(vo);
        dma16x4(vsrc, vo, vb);
        wait_vm<5>();
#pragma unroll
        for (int c = 0; c < 4; ++c) qf[c] = lds_read16(qaddr[c]);
        sa = sa1;
        sa1 = sa2;
        exp_range<12, 16>(ea, za);
        SB();
        __builtin_amdgcn_s_setprio(0);
        set_vo(so);
        exp_range<0, 2>(ebv, zb);
        SB();
        mfma_acc(ea, one, one);
        exp_range<2, 5>(ebv, zb);
        SB();
        mfma_new(za, ef, cw[0]);
        exp_range<5, 8>(ebv, zb);
        SB();
        mfma_acc(za, stf[0], pb[0][0]);
        arrived4(qf);
        dma16x4(qsrc, vo, qb);
        dma_e(eo, t + 4);
        exp_range<8, 11>(ebv, zb);
        SB();
        mfma_acc(za, stf[1], pb[0][1]);
        exp_range<11, 14>(ebv, zb);
        SB();
        mfma_acc(za, qf[0], sel[0]);
        exp_range<14, 16>(ebv, zb);
        SB();
        rcp_range<0, 1>(ea);
        SB();
        mfma_acc(ebv, one, one);
        rcp_range<1, 4>(ea);
        SB();
        mfma_acc(za, qf[1], sel[1]);
        rcp_range<4, 7>(ea);
        SB();
        mfma_new(zb, ef, cw[1]);
        rcp_range<7, 10>(ea);
        SB();
        mfma_acc(zb, stf[0], pb[1][0]);
        rcp_range<10, 13>(ea);
        SB();
        mfma_acc(zb, stf[1], pb[1][1]);
        rcp_range<13, 16>(ea);
        SB();
        mul16(ea, vf);
        SB();
        __builtin_amdgcn_s_setprio(1);
        mfma_new(vf, vfr[2], sel[0]);
        cvt16(m0, m1, ea);
        SB();
        mfma_acc(vf, vfr[3], sel[1]);
        rcp_range<0, 4>(ebv);
        SB();
        mfma_acc(agg[0], sf[0], __builtin_bit_cast(bf16x8, m0));
        rcp_range<4, 8>(ebv);
        SB();
        mfma_acc(agg[0], sf[1], __builtin_bit_cast(bf16x8, m1));
        rcp_range<8, 12>(ebv);
        SB();
        mfma_acc(zb, qf[2], sel[0]);
        rcp_range<12, 16>(ebv);
        SB();
        mul16(ebv, vf);
        SB();
      }
      __builtin_amdgcn_s_setprio(0);
      cvt16(m0, m1, ebv);
      agg[1] = mfma(sf[0], __builtin_bit_cast(bf16x8, m0), agg[1]);
      agg[1] = mfma(sf[1], __builtin_bit_cast(bf16x8, m1), agg[1]);
      s_write(sa, (u16)0);
      wait_vm0();
    }
    if (pass + 1 < npass) wg_barrier();
  }

  {
    const int lane = fresh_lane(), h = lane >> 5;
    const int blk = (lane >> 4) & 1, qq = (lane & 15) >> 2, p = lane & 3;
#pragma unroll
    for (int ct = 0; ct < 2; ++ct)
#pragma unroll
      for (int s = 0; s < 2; ++s) {
        s16x4 part[2];
#pragma unroll
        for (int t = 0; t < 2; ++t) {
          const int row = 16 * s + 8 * h + 4 * t + qq;
          const int ch = 4 * ct + 2 * blk + (p >> 1);
          part[t] = lds_read_tr(vb + row * 128 + ((ch ^ ((row >> 1) & 7)) << 4) + (p & 1) * 8);
        }
        agg[ct] = mfma(sel[s], join8(part[0], part[1]), agg[ct]);
      }
  }
  {
    const int lane = fresh_lane(), h = lane >> 5, f = lane & 31;
    const u32 obase = qb + 4 * h * 128 + f * 2;
#pragma unroll
    for (int ct = 0; ct < 2; ++ct) {
      f32x2 a[6], g[6];
#pragma unroll
      for (int i = 0; i < 6; ++i) {
        a[i] = (f32x2){agg[ct][2 * i], agg[ct][2 * i + 1]};
        const f32x2 y = a[i] * (f32x2){NLOG2E, NLOG2E};
        const f32x2 d = (f32x2){__builtin_amdgcn_exp2f(y.x), __builtin_amdgcn_exp2f(y.y)} + (f32x2){1.0f, 1.0f};
        g[i] = (f32x2){__builtin_amdgcn_rcpf(d.x), __builtin_amdgcn_rcpf(d.y)};
      }
#pragma unroll
      for (int i = 0; i < 6; ++i) {
        const int r = 2 * i;
        const f32x2 o = a[i] * g[i];
        const u32 pk = pack_bf16(o.x, o.y);
        const u32 ad = obase + ((r & 3) + 8 * (r >> 2)) * 128 + ct * 64;
        *(LDSP u16*)(size_t)ad = (u16)pk;
        *(LDSP u16*)(size_t)(ad + 128) = (u16)(pk >> 16);
      }
    }
    mem_fence();
    const rsrc_t outr = make_rsrc(out, (u32)N * D * 2);
#pragma unroll
    for (int i = 0; i < 3; ++i) {
      const int row = 8 * i + (lane >> 3);
      const u32x4 v = lds_read16u(qb + row * 128 + (lane & 7) * 16);
      if (row < tsz) store16_wt(outr, (u32)(n0 + row) * (D * 2) + slice * 128 + (lane & 7) * 16, v);
    }
  }
}

inline void launch_k1(void* const* d_in, void* d_ws, hipStream_t stream) {
  (void)hipFuncSetAttribute((const void*)prep_kernel, hipFuncAttributeMaxDynamicSharedMemorySize, K1_LDS);
  prep_kernel<<<NPROD + NGEMM, 512, K1_LDS, stream>>>((const char*)d_in[0], (const char*)d_in[1], (const int*)d_in[2],
                                                    (const int*)d_in[3], (const char*)d_in[4], (const char*)d_in[5],
                                                    (const char*)d_in[6], (char*)d_ws);
}
inline void launch_k2(void* const* d_in, void* d_out, void* d_ws, hipStream_t stream) {
  (void)hipFuncSetAttribute((const void*)edge_kernel, hipFuncAttributeMaxDynamicSharedMemorySize, K2_LDS);
  edge_kernel<<<256, 512, K2_LDS, stream>>>((const char*)d_ws, (u16*)d_out);
}

}

extern "C" void kernel_launch(void* const* d_in, const int* in_sizes, int n_in, void* d_out, int out_size,
                              void* d_ws, size_t ws_size, hipStream_t stream) {
  egc::launch_k1(d_in, d_ws, stream);
  egc::launch_k2(d_in, d_out, d_ws, stream);
}
```
